```python
import torch

def _softmax(x, axis=-1):
    return torch.softmax(x, dim=axis)
def _gelu(x):
    return torch.nn.functional.gelu(x, approximate="tanh")
def _silu(x):
    return torch.nn.functional.silu(x)
def _sigmoid(x):
    return torch.sigmoid(x)
def _layernorm(x, axis=-1, eps=1e-5):
    m = x.mean(dim=axis, keepdim=True)
    v = x.var(dim=axis, keepdim=True, unbiased=False)
    return (x - m) / torch.sqrt(v + eps)
def _scatter_add(tgt, idx, src, axis=0):
    return tgt.clone().index_add_(axis, idx, src)
def _segment_sum(src, seg, n):
    out = torch.zeros((n,) + src.shape[1:], dtype=src.dtype)
    return out.index_add_(0, seg, src)
def _pad(x, pw):
    flat = tuple(v for pair in reversed(pw) for v in pair)
    return torch.nn.functional.pad(x, flat)

kernel_name = 'gated_mix__softmax_full_mha__swiglu_ffn__prompt'

def setup_inputs():

    _w = (lambda a: (lambda t: t.to(torch.bfloat16) if t.is_floating_point() else t)(torch.as_tensor(a)))
    def _A_setup():
        B, S, D = 1, 2048, 448
        return {
            "x": _w((lambda *s: torch.randn(*s, dtype=torch.bfloat16) * 0.05)(B, S, D)),
            "wqkv": _w((lambda *s: torch.randn(*s, dtype=torch.bfloat16) * 0.8)(D, 3 * D)),
            "wo": _w((lambda *s: torch.randn(*s, dtype=torch.bfloat16) * 0.05)(D, D)),
        }
    _A_in = _A_setup()

    def _B_setup():
        B, S, D, F = 1, 2048, 448, 1792
        return {
            "x": _w((lambda *s: torch.randn(*s, dtype=torch.bfloat16) * 0.05)(B, S, D)),
            "w_gate": _w((lambda *s: torch.randn(*s, dtype=torch.bfloat16) * 0.1)(D, F)),
            "w_up": _w((lambda *s: torch.randn(*s, dtype=torch.bfloat16) * 0.1)(D, F)),
            "w_down": _w((lambda *s: torch.randn(*s, dtype=torch.bfloat16) * 0.1)(F, D)),
        }
    _B_in = _B_setup()

    return {
        "A_x": _A_in["x"],
        "A_wqkv": _A_in["wqkv"],
        "A_wo": _A_in["wo"],
        "B_x": _B_in["x"],
        "B_w_gate": _B_in["w_gate"],
        "B_w_up": _B_in["w_up"],
        "B_w_down": _B_in["w_down"],
        "gate_w": _w((lambda *s: torch.randn(*s, dtype=torch.bfloat16) * 0.05)(448) * 16.0),
    }

def reference(A_x, A_wqkv, A_wo, B_x, B_w_gate, B_w_up, B_w_down, gate_w):
    def _A_ref(x, wqkv, wo):
        B, S, D = x.shape
        H = 16; Dh = D // H
        qkv = (lambda x, *d: x.permute(*d))((x @ wqkv).reshape(B, S, 3, H, Dh), 2, 0, 3, 1, 4)
        q, k, v = qkv[0], qkv[1], qkv[2]
        scale = Dh ** -0.5
        scores = torch.einsum("bhqd,bhkd->bhqk", q, k) * scale
        p = _softmax(scores, axis=-1)
        o = torch.einsum("bhqk,bhkd->bhqd", p, v)
        return (lambda x, *d: x.permute(*d))(o, 0, 2, 1, 3).reshape(B, S, D) @ wo

    def _B_ref(x, w_gate, w_up, w_down):
        g = _silu(x @ w_gate)
        u = x @ w_up
        return (g * u) @ w_down

    x = A_x
    g = _sigmoid((x * gate_w).sum(-1, keepdims=True))
    a = _A_ref(x, A_wqkv, A_wo)
    b = _B_ref(x, B_w_gate, B_w_up, B_w_down)
    return g * a + (1 - g) * b
```

```cpp
#include <hip/hip_runtime.h>
#include <hip/hip_bf16.h>
#include <stdint.h>

#define DI __device__ __forceinline__
typedef short bf16x8 __attribute__((ext_vector_type(8)));
typedef short s16x4 __attribute__((ext_vector_type(4)));
typedef float f32x16 __attribute__((ext_vector_type(16)));
typedef float f32x4 __attribute__((ext_vector_type(4)));
typedef float f32x2 __attribute__((ext_vector_type(2)));
typedef __bf16 bf16x2 __attribute__((ext_vector_type(2)));
typedef unsigned u32x4 __attribute__((ext_vector_type(4)));
typedef unsigned u32x2 __attribute__((ext_vector_type(2)));
typedef __amdgpu_buffer_rsrc_t rsrc_t;
#define LAS __attribute__((address_space(3)))
typedef LAS char* lds_p;

constexpr int S_ = 2048, D_ = 448, F_ = 1792;
constexpr unsigned WS_GEN = 0, WS_FLAGA = 4096, WS_FLAGB = 8192, WS_G = 16384;
constexpr unsigned WS_KV = 65536;
constexpr unsigned WS_Q = WS_KV + (4u << 20);
constexpr unsigned WS_O = WS_Q + (2u << 20);
constexpr unsigned WS_H = WS_O + (2u << 20);
constexpr unsigned WS_END = WS_H + 64u * 112u * 1024u;
#define STAMP(i) do { } while (0)
constexpr int AUX_DEV = 16;
constexpr unsigned SENT = 0xFFFFFFFFu;
constexpr int U = 8192;
constexpr int LDS_A = 10 * U, LDS_B = 0, LDS_QX = 19 * U;

#define MFMA(a, b, c) __builtin_amdgcn_mfma_f32_32x32x16_bf16((a), (b), (c), 0, 0, 0)
#define SBAR() __builtin_amdgcn_sched_barrier(0)
#define PIN(x) asm volatile("" : "+v"(x))
#define WAITV(n) asm volatile("s_waitcnt vmcnt(" #n ")" ::: "memory")
#define BAR() asm volatile("s_waitcnt lgkmcnt(0)\n\ts_barrier" ::: "memory")
#define WAITV_BAR(n) asm volatile("s_waitcnt vmcnt(" #n ") lgkmcnt(0)\n\ts_barrier" ::: "memory")

DI unsigned rfl(unsigned x) { return __builtin_amdgcn_readfirstlane(x); }
DI int lane_now() { int l; asm volatile("v_mbcnt_lo_u32_b32 %0, -1, 0\n\tv_mbcnt_hi_u32_b32 %0, -1, %0" : "=v"(l)); return l; }
#define LANEVARS(sfx) const int lane##sfx = lane_now(), r##sfx = lane##sfx & 31, hh##sfx = lane##sfx >> 5, blk##sfx = (lane##sfx >> 4) & 1, q4##sfx = (lane##sfx >> 2) & 3, p4##sfx = lane##sfx & 3, swq##sfx = (q4##sfx >> 1) << 2
#define RELOAD() asm volatile("" ::: "memory")
DI unsigned cvtpk(float lo, float hi) { f32x2 v = {lo, hi}; return __builtin_bit_cast(unsigned, __builtin_convertvector(v, bf16x2)); }
DI bf16x8 asbf(u32x4 v) { return __builtin_bit_cast(bf16x8, v); }
DI rsrc_t mkrsrc(const void* p, unsigned bytes) { return __builtin_amdgcn_make_buffer_rsrc((void*)p, 0, bytes, 0x00020000); }
DI void dma16(rsrc_t rs, unsigned voff, unsigned soff, unsigned ldsaddr) {
  asm volatile("s_mov_b32 m0, %0\n\ts_nop 3\n\tbuffer_load_dwordx4 %1, %2, %3 offen lds" ::"s"(ldsaddr), "v"(voff), "s"(rs), "s"(soff) : "memory");
}
DI bf16x8 ldsr128(lds_p p) { return *(LAS bf16x8*)p; }
DI bf16x8 trfrag(lds_p p0, lds_p p1) {
  typedef short v4s __attribute__((ext_vector_type(4)));
  v4s a = __builtin_amdgcn_ds_read_tr16_b64_v4i16((LAS v4s*)p0), b = __builtin_amdgcn_ds_read_tr16_b64_v4i16((LAS v4s*)p1);
  return __builtin_shufflevector(a, b, 0, 1, 2, 3, 4, 5, 6, 7);
}
DI u32x4 gld(rsrc_t rs, unsigned voff, unsigned soff) { return __builtin_amdgcn_raw_buffer_load_b128(rs, voff, soff, AUX_DEV); }
DI void gst(rsrc_t rs, u32x4 v, unsigned voff, unsigned soff) { __builtin_amdgcn_raw_buffer_store_b128(v, rs, voff, soff, AUX_DEV); }
DI void gst8(rsrc_t rs, u32x2 v, unsigned voff, unsigned soff) { __builtin_amdgcn_raw_buffer_store_b64(v, rs, voff, soff, AUX_DEV); }
DI u32x4 pack8(const f32x16& a, int s) { return (u32x4){cvtpk(a[8 * s], a[8 * s + 1]), cvtpk(a[8 * s + 2], a[8 * s + 3]), cvtpk(a[8 * s + 4], a[8 * s + 5]), cvtpk(a[8 * s + 6], a[8 * s + 7])}; }
DI float halfmax(float m) { auto t = __builtin_amdgcn_permlane32_swap(__float_as_uint(m), __float_as_uint(m), false, false); return __builtin_fmaxf(__uint_as_float(t[0]), __uint_as_float(t[1])); }
DI float max16(const f32x16& s) {
  float a = __builtin_fmaxf(s[0], s[1]);
#pragma unroll
  for (int i = 2; i < 16; i += 2) a = __builtin_fmaxf(__builtin_fmaxf(a, s[i]), s[i + 1]);
  return a;
}
constexpr unsigned long long SPIN_TICKS = 20000000ull;

DI u32x4 swiglu(const f32x16& a) {
  float h[8];
#pragma unroll
  for (int i = 0; i < 8; ++i) { const float e = __builtin_amdgcn_exp2f(a[i] * -1.4426950408889634f); h[i] = a[i] * __builtin_amdgcn_rcpf(1.f + e) * a[i + 8]; }
  return (u32x4){cvtpk(h[0], h[1]), cvtpk(h[2], h[3]), cvtpk(h[4], h[5]), cvtpk(h[6], h[7])};
}
template <bool ISQ>
DI void epi_qk(rsrc_t rw, const f32x16& a, int g4base, int T, int r, int hh) {
  const float c = ISQ ? 0.18898223650461362f * 1.4426950408889634f : 1.f;
#pragma unroll
  for (int g = 0; g < 4; ++g) {
    const unsigned G4 = g4base + 2 * g + hh, head = (G4 * 9363u) >> 16, dg = G4 - 7 * head;
    const unsigned off = (ISQ ? WS_Q : WS_KV) + (head * 64 + T) * (ISQ ? 2048u : 4096u) + (dg >> 2) * 1024 + (r + 32 * ((dg >> 1) & 1)) * 16 + (dg & 1) * 8;
    gst8(rw, (u32x2){cvtpk(a[4 * g] * c, a[4 * g + 1] * c), cvtpk(a[4 * g + 2] * c, a[4 * g + 3] * c)}, off, 0);
    if (!ISQ && dg == 6) gst8(rw, (u32x2){0x3F803F80u, 0x00003F80u}, off + 8, 0);
  }
}
DI void epi_v(rsrc_t rw, const f32x16& a, int vcbase, int T, int r, int hh) {
  const unsigned vc = vcbase + r, head = (vc * 2341u) >> 16, d = vc - 28 * head;
  const unsigned off = WS_KV + (head * 64 + T) * 4096u + 2048u + (d + 32 * hh) * 16;
  gst(rw, pack8(a, 0), off, 0); gst(rw, pack8(a, 1), off + 1024, 0);
  if (d >= 24) { const unsigned v = d == 24 ? 0x3F803F80u : 0u; const u32x4 pv = {v, v, v, v}; gst(rw, pv, off + 64, 0); gst(rw, pv, off + 1024 + 64, 0); }
}

__global__ __launch_bounds__(512, 2) void fused_kernel(const void* __restrict__ xg, const void* __restrict__ wqkv, const void* __restrict__ wo,
                                                       const void* __restrict__ wg, const void* __restrict__ wu, const void* __restrict__ wd,
                                                       const void* __restrict__ gw, void* __restrict__ out, char* __restrict__ ws) {
  __shared__ __attribute__((aligned(16))) char smem[163840];
  const lds_p L = (lds_p)smem;
  const unsigned L0 = (unsigned)(uintptr_t)L;
  const int tid = threadIdx.x, lane = tid & 63, w = rfl(tid >> 6);
  const int r = lane & 31, hh = lane >> 5, blk = (lane >> 4) & 1, q4 = (lane >> 2) & 3, p4 = lane & 3;
  const int bid = blockIdx.x, xcd = bid & 7, idx = bid >> 3;
  const rsrc_t rw = mkrsrc(ws, WS_END);
  STAMP(0);

  const int rb = (xcd & 1) * 8 + (idx & 7), cs = (xcd >> 1) * 4 + (idx >> 3);
  const bool isQ = cs >= 14, isV = cs >= 7 && cs < 14;
  const int rt = w & 3, half = w >> 2, T = rb * 4 + rt;
  const int head = 2 * xcd + (idx >> 4), qb = idx & 15;
  const bool hasDown = idx < 28;
  const int ti = hasDown ? idx : idx - 28, rb64 = 4 * xcd + ti / 7, cb = ti % 7;
  const int dr = lane >> 3, dp = lane & 7, swW = ((dr >> 1) & 1) << 2;
  const rsrc_t rx = mkrsrc(xg, S_ * D_ * 2), rq = mkrsrc(wqkv, D_ * 3 * D_ * 2);
  const rsrc_t r3 = mkrsrc(isQ ? wqkv : wg, isQ ? D_ * 3 * D_ * 2 : D_ * F_ * 2), r4 = mkrsrc(isQ ? wqkv : wu, isQ ? D_ * 3 * D_ * 2 : D_ * F_ * 2);
  const unsigned vx = (8 * w + dr) * 896 + ((dp ^ (((w & 1) << 2) | (dr >> 1))) << 4);
  const unsigned v2 = (8 * w + dr) * 2688 + ((dp ^ swW) << 4);
  const unsigned v3 = isQ ? v2 : (8 * w + dr) * 3584 + ((dp ^ swW) << 4);
  const unsigned v4 = isQ ? v2 : (8 * w + dr) * 3584 + ((dp ^ swW ^ 2) << 4);
  const int lc0 = isQ ? (cs == 14 ? 0 : 4) : 7 + cs;
  const unsigned sx = rb * 128 * 896, s2 = lc0 * 128, s3 = isQ ? s2 + 128 : cs * 256, s4 = isQ ? s2 + 256 : cs * 256;
  const unsigned st34 = isQ ? 64 * 2688 : 64 * 3584;
  const unsigned ldw = L0 + w * 1024;
  auto issue1 = [&](int c, int slot) {
    dma16(rx, vx, sx + c * 128, ldw + slot * U);
    dma16(rx, vx, sx + 64 * 896 + c * 128, ldw + (slot + 1) * U);
    dma16(rq, v2, s2 + c * 64 * 2688, ldw + (slot + 2) * U);
    dma16(r3, v3, s3 + c * st34, ldw + (slot + 3) * U);
    dma16(r4, v4, s4 + c * st34, ldw + (slot + 4) * U);
  };
  const unsigned s3b = isQ ? 3 * 128 : cs * 256 + 128;
  auto issue2 = [&](int c, int slot) {
    dma16(r3, v3, s3b + c * st34, ldw + slot * U);
    dma16(r4, v4, s3b + c * st34, ldw + (slot + 1) * U);
  };
  issue1(0, 0); issue1(1, 5); issue1(2, 10); issue1(3, 15);
  const int rowp = (8 * hh + q4) * 128 + (p4 & 1) * 8, swq = (q4 >> 1) << 2;
  const lds_p A0 = L + rowp + (((4 * half + 2 * blk + (p4 >> 1)) ^ swq) << 4);
  const lds_p A1 = isQ ? A0 : L + blk * U + rowp + (((4 * half + (p4 >> 1)) ^ swq ^ (blk << 1)) << 4);
  const lds_p A2 = isQ ? A0 + U : L + blk * U + rowp + (((4 * half + 2 + (p4 >> 1)) ^ swq ^ (blk << 1)) << 4);
  const int xb = (rt >> 1) * U + ((rt & 1) * 32 + r) * 128, xs = (hh ^ ((r >> 1) & 7)) << 4;
  bf16x8 xf[28];
  f32x16 a0 = {}, a1 = {}, a2 = {};
#define S1CHUNK(c, slot, SWAP) _Pragma("unroll") for (int k = 0; k < 4; ++k) { \
    xf[4 * (c) + k] = ldsr128(L + (slot) * U + xb + (xs ^ (k * 32))); \
    const bf16x8 w0 = trfrag(A0 + ((slot) + 2) * U + k * 2048, A0 + ((slot) + 2) * U + k * 2048 + 512); \
    const bf16x8 w1 = trfrag(A1 + ((slot) + 3) * U + k * 2048, A1 + ((slot) + 3) * U + k * 2048 + 512); \
    const bf16x8 w2 = trfrag(A2 + ((slot) + 3) * U + k * 2048, A2 + ((slot) + 3) * U + k * 2048 + 512); \
    a0 = SWAP ? MFMA(xf[4 * (c) + k], w0, a0) : MFMA(w0, xf[4 * (c) + k], a0); \
    a1 = MFMA(w1, xf[4 * (c) + k], a1); a2 = MFMA(w2, xf[4 * (c) + k], a2); }
#define STAGE1(SWAP) \
  WAITV_BAR(15); S1CHUNK(0, 0, SWAP) \
  WAITV_BAR(10); issue1(4, 0); S1CHUNK(1, 5, SWAP) \
  WAITV_BAR(10); issue1(5, 5); S1CHUNK(2, 10, SWAP) \
  WAITV_BAR(10); issue1(6, 10); S1CHUNK(3, 15, SWAP) \
  WAITV_BAR(10); issue2(0, 15); issue2(1, 17); S1CHUNK(4, 0, SWAP) \
  WAITV_BAR(9); issue2(2, 0); issue2(3, 2); S1CHUNK(5, 5, SWAP) \
  WAITV_BAR(8); issue2(4, 4); issue2(5, 6); issue2(6, 8); S1CHUNK(6, 10, SWAP)
  if (isV) { STAGE1(true) } else { STAGE1(false) }
  STAMP(1);
  const rsrc_t rd = mkrsrc(wd, F_ * D_ * 2);
  const unsigned vd = (8 * w + dr) * 896 + ((dp ^ swW) << 4);
  auto issue_wd = [&](int b, int ldsoff) { dma16(rd, vd, (128 * (b % 14) + 64 * (b / 14)) * 896 + cb * 128, ldw + ldsoff); };
  unsigned ep0, ep1;
  u32x2 genv;
  const unsigned offO = WS_O + (((qb * 4 + (w >> 1)) * 16 + head) * 2 + (w & 1)) * 1024 + lane * 16;
  auto pre2 = [&]() {
    genv = __builtin_amdgcn_raw_buffer_load_b64(rw, WS_GEN, 0, AUX_DEV);
    gst(rw, (u32x4){SENT, SENT, SENT, SENT}, offO, 0); };
  auto setep = [&]() { PIN(genv); const unsigned long long e = (((unsigned long long)rfl(genv[1]) << 32) | rfl(genv[0])) + 1ull; ep0 = (unsigned)e; ep1 = (unsigned)(e >> 32); };
  auto publish = [&](unsigned flagoff) { if (tid == 0) gst8(rw, (u32x2){ep0, ep1}, flagoff + bid * 8, 0); };
  const unsigned offH = WS_H + (T * 112 + cs * 4 + 2 * half) * 1024 + lane * 16;
  f32x16 b1 = {}, b2 = {};
#define S2CHUNK(c, slot) _Pragma("unroll") for (int k = 0; k < 4; ++k) { \
    const bf16x8 w1 = trfrag(A1 + (slot) * U + k * 2048, A1 + (slot) * U + k * 2048 + 512); \
    const bf16x8 w2 = trfrag(A2 + (slot) * U + k * 2048, A2 + (slot) * U + k * 2048 + 512); \
    b1 = MFMA(w1, xf[4 * (c) + k], b1); b2 = MFMA(w2, xf[4 * (c) + k], b2); }
#define S2QCHUNK(c, slot) _Pragma("unroll") for (int k = 0; k < 4; ++k) { \
    const bf16x8 w1 = trfrag(A1 + (slot) * U + k * 2048, A1 + (slot) * U + k * 2048 + 512); \
    b1 = MFMA(w1, xf[4 * (c) + k], b1); }
  if (!isQ) {
    WAITV_BAR(8);
    pre2();
    if (isV) epi_v(rw, a0, 64 * (cs - 7) + 32 * half, T, r, hh); else epi_qk<false>(rw, a0, 16 * cs + 8 * half, T, r, hh);
    gst(rw, swiglu(a1), offH, 0); gst(rw, swiglu(a2), offH + 1024, 0);
    S2CHUNK(0, 15) S2CHUNK(1, 17) S2CHUNK(2, 0)
    WAITV_BAR(0);
    setep(); publish(WS_FLAGA);
#pragma unroll
    for (int b = 0; b < 8; ++b) issue_wd(b, LDS_A + b * U);
    S2CHUNK(3, 2) S2CHUNK(4, 4) S2CHUNK(5, 6) S2CHUNK(6, 8)
  } else {
    epi_qk<true>(rw, a0, 16 * lc0 + 8 * half, T, r, hh); epi_qk<true>(rw, a1, 16 * lc0 + 16 + 8 * half, T, r, hh); epi_qk<true>(rw, a2, 16 * lc0 + 32 + 8 * half, T, r, hh);
    WAITV_BAR(12);
    pre2();
    if (cs == 14) {
      S2QCHUNK(0, 15) S2QCHUNK(1, 17) S2QCHUNK(2, 0) S2QCHUNK(3, 2) S2QCHUNK(4, 4) S2QCHUNK(5, 6) S2QCHUNK(6, 8)
      epi_qk<true>(rw, b1, 48 + 8 * half, T, r, hh);
    } else if (half == 0) {
#pragma unroll
      for (int k = 0; k < 28; ++k) { const bf16x8 gf = *(const bf16x8*)((const char*)gw + k * 32 + hh * 16); b1 = MFMA(gf, xf[k], b1); }
      const float g = __builtin_amdgcn_rcpf(1.f + __builtin_amdgcn_exp2f(b1[0] * -1.4426950408889634f));
      if (hh == 0) __builtin_amdgcn_raw_buffer_store_b32(__float_as_uint(g), rw, WS_G + (T * 32 + r) * 4, 0, AUX_DEV);
    }
    WAITV_BAR(0);
    setep(); publish(WS_FLAGA);
#pragma unroll
    for (int b = 0; b < 8; ++b) issue_wd(b, LDS_A + b * U);
  }
  STAMP(2);

  { const unsigned long long t0 = __builtin_amdgcn_s_memrealtime();
    for (;;) {
      RELOAD();
      const u32x4 f0 = gld(rw, WS_FLAGA + lane * 32, 0), f1 = gld(rw, WS_FLAGA + lane * 32 + 16, 0);
      const bool ok = f0[0] == ep0 && f0[1] == ep1 && f0[2] == ep0 && f0[3] == ep1 && f1[0] == ep0 && f1[1] == ep1 && f1[2] == ep0 && f1[3] == ep1;
      if (__all(ok) || __builtin_amdgcn_s_memrealtime() - t0 > SPIN_TICKS) break;
      __builtin_amdgcn_s_sleep(2);
    }
    asm volatile("" ::: "memory"); }
  if (bid == 0 && tid == 0) gst8(rw, (u32x2){ep0, ep1}, WS_GEN, 0);
  STAMP(3);
  const unsigned l16 = lane * 16;
  const unsigned kvb = WS_KV + (head * 64 + 8 * w) * 4096;
  u32x4 kA0 = gld(rw, l16, kvb), kA1 = gld(rw, l16, kvb + 1024);
  u32x4 qx0, qx1;
  if (w < 4) { qx0 = gld(rw, l16, WS_Q + (head * 64 + qb * 4 + w) * 2048); qx1 = gld(rw, l16, WS_Q + (head * 64 + qb * 4 + w) * 2048 + 1024); }
  u32x4 vA0 = gld(rw, l16, kvb + 2048), vA1 = gld(rw, l16, kvb + 3072);
  u32x4 kB0 = gld(rw, l16, kvb + 4096), kB1 = gld(rw, l16, kvb + 5120), vB0 = gld(rw, l16, kvb + 6144), vB1 = gld(rw, l16, kvb + 7168);
  if (!isQ) { gst(rw, swiglu(b1), offH + 56 * 1024, 0); gst(rw, swiglu(b2), offH + 57 * 1024, 0); }
  if (w < 4) {
    if (hh) { qx1[2] = 0; qx1[3] = 0; }
    f32x16 s = MFMA(asbf(kA0), asbf(qx0), f32x16{}); s = MFMA(asbf(kA1), asbf(qx1), s);
    const float m = -halfmax(max16(s));
    const unsigned m0 = __float_as_uint(m) & 0xFFFF0000u; const float e1 = m - __uint_as_float(m0);
    const unsigned m1 = __float_as_uint(e1) & 0xFFFF0000u; const float e2 = e1 - __uint_as_float(m1);
    if (hh) { qx1[2] = (m0 >> 16) | m1; qx1[3] = __float_as_uint(e2) >> 16; }
    *(LAS u32x4*)(L + LDS_QX + w * 2048 + l16) = qx0; *(LAS u32x4*)(L + LDS_QX + w * 2048 + 1024 + l16) = qx1;
  }
  BAR();
  bf16x8 qf[4][2];
#pragma unroll
  for (int i = 0; i < 4; ++i) { qf[i][0] = ldsr128(L + LDS_QX + i * 2048 + l16); qf[i][1] = ldsr128(L + LDS_QX + i * 2048 + 1024 + l16); }
  const int rt2 = w & 1, kq = w >> 1;
  const unsigned hb = WS_H + ((rb64 * 2 + rt2) * 112 + kq) * 1024;
  u32x4 hf[4];
#pragma unroll
  for (int i = 0; i < 4; ++i) hf[i] = gld(rw, l16, hb + i * 4096);
#pragma unroll
  for (int b = 0; b < 8; ++b) issue_wd(8 + b, LDS_B + b * U);
  issue_wd(24, 8 * U); issue_wd(25, 9 * U); issue_wd(26, 18 * U);
  const int wrow = (16 * kq + 4 * hh + q4) * 128 + (p4 & 1) * 8;
  const lds_p W0 = L + wrow + (((2 * blk + (p4 >> 1)) ^ swq) << 4), W1 = L + wrow + (((4 + 2 * blk + (p4 >> 1)) ^ swq) << 4);
  f32x16 o0 = {}, o1 = {}, o2 = {}, o3 = {}, d0 = {}, d1 = {}, sA, sB;
  u32x4 p0 = {}, p1 = {};
  STAMP(4);
#define EX4(X, i) X[i] = __builtin_amdgcn_exp2f(X[i]); X[i + 1] = __builtin_amdgcn_exp2f(X[i + 1]); X[i + 2] = __builtin_amdgcn_exp2f(X[i + 2]); X[i + 3] = __builtin_amdgcn_exp2f(X[i + 3]);
#define STEP(CUR, NXT, K0, K1, QN, V0, V1, OP) \
    NXT = MFMA(asbf(K0), QN[0], f32x16{}); EX4(CUR, 0) PIN(CUR); SBAR(); \
    OP = MFMA(asbf(V0), asbf(p0), OP); EX4(CUR, 4) PIN(CUR); SBAR(); \
    NXT = MFMA(asbf(K1), QN[1], NXT); EX4(CUR, 8) p0 = pack8(CUR, 0); PIN(CUR); PIN(p0); SBAR(); \
    OP = MFMA(asbf(V1), asbf(p1), OP); EX4(CUR, 12) p1 = pack8(CUR, 1); PIN(p1); SBAR();
#define DOWN(i, base, nb) { \
    const bf16x8 wa = trfrag(W0 + (base), W0 + (base) + 1024), wb = trfrag(W1 + (base), W1 + (base) + 1024); \
    d0 = MFMA(wa, asbf(hf[(i) & 3]), d0); d1 = MFMA(wb, asbf(hf[(i) & 3]), d1); \
    hf[(i) & 3] = gld(rw, l16, hb + (nb) * 4096); SBAR(); }
  sA = MFMA(asbf(kA0), qf[0][0], f32x16{}); sA = MFMA(asbf(kA1), qf[0][1], sA);
  STEP(sA, sB, kA0, kA1, qf[1], vA0, vA1, o3) STEP(sB, sA, kA0, kA1, qf[2], vA0, vA1, o0) STEP(sA, sB, kA0, kA1, qf[3], vA0, vA1, o1)
  kA0 = gld(rw, l16, kvb + 8192); kA1 = gld(rw, l16, kvb + 8192 + 1024);
  STEP(sB, sA, kB0, kB1, qf[0], vA0, vA1, o2)
#pragma unroll 1
  for (int p = 0; p < 3; ++p) {
    WAITV_BAR(2);
    if (p == 0) { publish(WS_FLAGB); issue_wd(27, 19 * U); }
    if (p == 1) {
#pragma unroll
      for (int b = 0; b < 8; ++b) issue_wd(16 + b, LDS_A + b * U);
      const int prb = rb64 >> 1, pcs = lane < 14 ? lane : 0;
      const unsigned fo = WS_FLAGB + ((((prb & 7) + 8 * (pcs & 3)) << 3) + (prb >> 3) + 2 * (pcs >> 2)) * 8;
      const unsigned long long t0 = __builtin_amdgcn_s_memrealtime();
      for (;;) {
        RELOAD();
        const u32x2 f = __builtin_amdgcn_raw_buffer_load_b64(rw, fo, 0, AUX_DEV);
        if (__all(f[0] == ep0 && f[1] == ep1) || __builtin_amdgcn_s_memrealtime() - t0 > SPIN_TICKS) break;
        __builtin_amdgcn_s_sleep(2);
      }
      asm volatile("" ::: "memory");
    }
    const int lb = (p & 1) ? LDS_B : LDS_A, nb = 8 * p + 4;
    const unsigned kn = kvb + (2 * p + 2) * 4096;
    STEP(sA, sB, kB0, kB1, qf[1], vA0, vA1, o3)
    vA0 = gld(rw, l16, kn + 2048); vA1 = gld(rw, l16, kn + 3072);
    DOWN(0, lb, nb)
    STEP(sB, sA, kB0, kB1, qf[2], vB0, vB1, o0) DOWN(1, lb + U, nb + 1)
    STEP(sA, sB, kB0, kB1, qf[3], vB0, vB1, o1)
    kB0 = gld(rw, l16, kn + 4096); kB1 = gld(rw, l16, kn + 5120);
    DOWN(2, lb + 2 * U, nb + 2)
    STEP(sB, sA, kA0, kA1, qf[0], vB0, vB1, o2) DOWN(3, lb + 3 * U, nb + 3)
    STEP(sA, sB, kA0, kA1, qf[1], vB0, vB1, o3)
    vB0 = gld(rw, l16, kn + 6144); vB1 = gld(rw, l16, kn + 7168);
    DOWN(4, lb + 4 * U, nb + 4)
    STEP(sB, sA, kA0, kA1, qf[2], vA0, vA1, o0) DOWN(5, lb + 5 * U, nb + 5)
    STEP(sA, sB, kA0, kA1, qf[3], vA0, vA1, o1)
    { const unsigned kk = p == 2 ? 0x40000000u : kn + 8192; kA0 = gld(rw, l16, kk); kA1 = gld(rw, l16, kk + 1024); }
    DOWN(6, lb + 6 * U, nb + 6)
    STEP(sB, sA, kB0, kB1, qf[0], vA0, vA1, o2) DOWN(7, lb + 7 * U, nb + 7)
  }
  BAR();
  const lds_p PW = L + (w < 4 ? LDS_B : LDS_A) + (w & 3) * 16384 + l16;
#define PUTO(O, qt) { *(LAS f32x4*)(PW + (qt) * 4096) = O.s0123; *(LAS f32x4*)(PW + (qt) * 4096 + 1024) = O.s4567; *(LAS f32x4*)(PW + (qt) * 4096 + 2048) = O.s89ab; *(LAS f32x4*)(PW + (qt) * 4096 + 3072) = O.scdef; }
  const float gq = __uint_as_float(__builtin_amdgcn_raw_buffer_load_b32(rw, WS_G + ((qb * 4 + (w >> 1)) * 32 + r) * 4, 0, AUX_DEV));
  const float gd = __uint_as_float(__builtin_amdgcn_raw_buffer_load_b32(rw, WS_G + ((rb64 * 2 + rt2) * 32 + r) * 4, 0, AUX_DEV));
  STEP(sA, sB, kB0, kB1, qf[1], vA0, vA1, o3)
  STEP(sB, sA, kB0, kB1, qf[2], vB0, vB1, o0)
  STEP(sA, sB, kB0, kB1, qf[3], vB0, vB1, o1) PUTO(o0, 0)
  { o2 = MFMA(asbf(vB0), asbf(p0), o2); o2 = MFMA(asbf(vB1), asbf(p1), o2); EX4(sB, 0) EX4(sB, 4) EX4(sB, 8) EX4(sB, 12) PUTO(o1, 1)
    p0 = pack8(sB, 0); p1 = pack8(sB, 1); o3 = MFMA(asbf(vB0), asbf(p0), o3); o3 = MFMA(asbf(vB1), asbf(p1), o3); PUTO(o2, 2) PUTO(o3, 3) }
  STAMP(5);
  bool stored = false;
#pragma unroll 1
  for (int attempt = 0;; ++attempt) {
    BAR();
    f32x4 x0 = {}, x1 = {}; float l = 0.f;
#pragma unroll
    for (int sw = 0; sw < 8; ++sw) {
      const lds_p PR = L + (sw < 4 ? LDS_B : LDS_A) + (sw & 3) * 16384 + (w >> 1) * 4096;
      x0 += *(LAS f32x4*)(PR + (w & 1) * 2048 + l16); x1 += *(LAS f32x4*)(PR + (w & 1) * 2048 + 1024 + l16);
      l += *(LAS float*)(PR + 3072 + (r + 32) * 16);
    }
    const float am = __builtin_fmaxf(__builtin_fmaxf(__builtin_fmaxf(__builtin_fabsf(x0[0]) + __builtin_fabsf(x0[1]), __builtin_fabsf(x0[2]) + __builtin_fabsf(x0[3])),
                                                     __builtin_fabsf(x1[0]) + __builtin_fabsf(x1[1])), __builtin_fabsf(x1[2]) + __builtin_fabsf(x1[3]));
    const float chk = (x0[0] + x0[1] + x0[2] + x0[3] + x1[0] + x1[1] + x1[2] + x1[3]) * 0.f;
    const bool good = l > 0x1p-60f && l < 0x1p60f && am < 0x1p120f && chk == 0.f;
    const bool ok = attempt == 1 || __all(good);
    if (ok && !stored) {
      const float sc = gq * __builtin_amdgcn_rcpf(l);
      u32x4 ov = {cvtpk(x0[0] * sc, x0[1] * sc), cvtpk(x0[2] * sc, x0[3] * sc), cvtpk(x1[0] * sc, x1[1] * sc), cvtpk(x1[2] * sc, x1[3] * sc)};
      if ((w & 1) && hh) { ov[2] = 0; ov[3] = 0; }
#pragma unroll
      for (int i = 0; i < 4; ++i) ov[i] = ov[i] == SENT ? 0x7FC07FC0u : ov[i];
      gst(rw, ov, offO, 0);
      stored = true;
    }
    LAS float* const redo = (LAS float*)(L + LDS_A + 3072 + 512 + 4);
    if (!ok && lane == 0) *redo = 1.f;
    BAR();
    if (attempt == 1 || rfl(__float_as_uint(*redo)) == 0u) break;
#pragma unroll 1
    for (int qt = 0; qt < 4; ++qt) {
      const u32x4 q0 = gld(rw, l16, WS_Q + (head * 64 + qb * 4 + qt) * 2048); u32x4 q1 = gld(rw, l16, WS_Q + (head * 64 + qb * 4 + qt) * 2048 + 1024);
      if (hh) { q1[2] = 0; q1[3] = 0; }
      float m = -__builtin_inff();
#pragma unroll 1
      for (int kt = 0; kt < 8; ++kt) {
        f32x16 s = MFMA(asbf(gld(rw, l16, kvb + kt * 4096)), asbf(q0), f32x16{}); s = MFMA(asbf(gld(rw, l16, kvb + kt * 4096 + 1024)), asbf(q1), s);
        m = __builtin_fmaxf(m, max16(s));
      }
      m = halfmax(m);
      *(LAS float*)(PW + qt * 4096) = m;
      BAR();
#pragma unroll
      for (int sw = 0; sw < 8; ++sw) m = __builtin_fmaxf(m, *(LAS float*)(L + (sw < 4 ? LDS_B : LDS_A) + (sw & 3) * 16384 + qt * 4096 + l16));
      BAR();
      f32x16 o = {};
#pragma unroll 1
      for (int kt = 0; kt < 8; ++kt) {
        f32x16 s = MFMA(asbf(gld(rw, l16, kvb + kt * 4096)), asbf(q0), f32x16{}); s = MFMA(asbf(gld(rw, l16, kvb + kt * 4096 + 1024)), asbf(q1), s);
#pragma unroll
        for (int i = 0; i < 16; ++i) s[i] = __builtin_amdgcn_exp2f(s[i] - m);
        o = MFMA(asbf(gld(rw, l16, kvb + kt * 4096 + 2048)), asbf(pack8(s, 0)), o); o = MFMA(asbf(gld(rw, l16, kvb + kt * 4096 + 3072)), asbf(pack8(s, 1)), o);
      }
      PUTO(o, qt)
    }
  }
  STAMP(6);
  if (!hasDown) return;

  { const rsrc_t ro = mkrsrc(wo, D_ * D_ * 2);
#pragma unroll
    for (int b = 0; b < 7; ++b) dma16(ro, vd, b * 64 * 896 + cb * 128, ldw + LDS_B + b * U); }
#define DOWN3(i, base) { const bf16x8 wa = trfrag(W0 + (base), W0 + (base) + 1024), wb = trfrag(W1 + (base), W1 + (base) + 1024); \
    d0 = MFMA(wa, asbf(hf[i]), d0); d1 = MFMA(wb, asbf(hf[i]), d1); }
  DOWN3(0, 8 * U) DOWN3(1, 9 * U) DOWN3(2, 18 * U) DOWN3(3, 19 * U)
  d0 *= 1.f - gd; d1 *= 1.f - gd;
  u32x4 of[8];
  { const unsigned ob = WS_O + ((rb64 * 2 + rt2) * 32 + kq * 8) * 1024; const unsigned long long t0 = __builtin_amdgcn_s_memrealtime();
    for (;;) {
      RELOAD();
      bool ok = true;
#pragma unroll
      for (int i = 0; i < 8; ++i) of[i] = gld(rw, l16, ob + i * 1024);
#pragma unroll
      for (int i = 0; i < 8; ++i) ok = ok && of[i][0] != SENT && of[i][1] != SENT && of[i][2] != SENT && of[i][3] != SENT;
      if (__all(ok) || __builtin_amdgcn_s_memrealtime() - t0 > SPIN_TICKS) break;
      __builtin_amdgcn_s_sleep(1);
    } }
  STAMP(7);
  WAITV_BAR(0);
  LANEVARS(c);
  { const int orow = (112 * kq + 4 * hhc + q4c) * 128 + (p4c & 1) * 8, fix = hhc ? 512 : 0;
    const lds_p X0 = L + LDS_B + orow + (((2 * blkc + (p4c >> 1)) ^ swqc) << 4), X1 = L + LDS_B + orow + (((4 + 2 * blkc + (p4c >> 1)) ^ swqc) << 4);
#pragma unroll
    for (int h = 0; h < 4; ++h) {
      const int o = h * 28 * 128;
      d0 = MFMA(trfrag(X0 + o, X0 + o + 1024), asbf(of[2 * h]), d0); d1 = MFMA(trfrag(X1 + o, X1 + o + 1024), asbf(of[2 * h]), d1);
      d0 = MFMA(trfrag(X0 + o + 2048, X0 + o + 3072 - fix), asbf(of[2 * h + 1]), d0); d1 = MFMA(trfrag(X1 + o + 2048, X1 + o + 3072 - fix), asbf(of[2 * h + 1]), d1);
    } }
  { const lds_p R = L + LDS_A + kq * 16384 + (rt2 * 32 + rc) * 256; const int sw = rc & 7;
    *(LAS f32x4*)(R + ((hhc ^ sw) << 4)) = d0.s0123;       *(LAS f32x4*)(R + (((2 + hhc) ^ sw) << 4)) = d0.s4567;
    *(LAS f32x4*)(R + (((4 + hhc) ^ sw) << 4)) = d0.s89ab; *(LAS f32x4*)(R + (((6 + hhc) ^ sw) << 4)) = d0.scdef;
    *(LAS f32x4*)(R + 128 + ((hhc ^ sw) << 4)) = d1.s0123;       *(LAS f32x4*)(R + 128 + (((2 + hhc) ^ sw) << 4)) = d1.s4567;
    *(LAS f32x4*)(R + 128 + (((4 + hhc) ^ sw) << 4)) = d1.s89ab; *(LAS f32x4*)(R + 128 + (((6 + hhc) ^ sw) << 4)) = d1.scdef; }
  BAR();
  { const int row = w * 8 + (lanec >> 3), c8 = lanec & 7, sw = row & 7; f32x4 y0 = {}, y1 = {};
    const lds_p R = L + LDS_A + row * 256 + (c8 >> 2) * 128;
#pragma unroll
    for (int k = 0; k < 4; ++k) { y0 += *(LAS f32x4*)(R + k * 16384 + ((((2 * c8) & 7) ^ sw) << 4)); y1 += *(LAS f32x4*)(R + k * 16384 + ((((2 * c8 + 1) & 7) ^ sw) << 4)); }
    *(u32x4*)((char*)out + (size_t)(rb64 * 64 + row) * 896 + cb * 128 + c8 * 16) = (u32x4){cvtpk(y0[0], y0[1]), cvtpk(y0[2], y0[3]), cvtpk(y1[0], y1[1]), cvtpk(y1[2], y1[3])}; }
  STAMP(8);
}

extern "C" void kernel_launch(void* const* d_in, const int* in_sizes, int n_in, void* d_out, int out_size, void* d_ws, size_t ws_size,
                              hipStream_t stream) {
  (void)in_sizes; (void)n_in; (void)out_size; (void)ws_size;
  fused_kernel<<<256, 512, 0, stream>>>(d_in[0], d_in[1], d_in[2], d_in[4], d_in[5], d_in[6], d_in[7], d_out, (char*)d_ws);
}
```

```cpp
#include <hip/hip_runtime.h>
#include <hip/hip_bf16.h>
#include <stdint.h>

#define DI __device__ __forceinline__
typedef short bf16x8 __attribute__((ext_vector_type(8)));
typedef float f32x16 __attribute__((ext_vector_type(16)));
typedef float f32x4 __attribute__((ext_vector_type(4)));
typedef float f32x2 __attribute__((ext_vector_type(2)));
typedef __bf16 bf16x2 __attribute__((ext_vector_type(2)));
typedef unsigned u32x4 __attribute__((ext_vector_type(4)));
typedef unsigned u32x2 __attribute__((ext_vector_type(2)));
typedef __amdgpu_buffer_rsrc_t rsrc_t;
#define LAS __attribute__((address_space(3)))
typedef LAS char* lds_p;

constexpr int S_ = 2048, D_ = 448, F_ = 1792;
constexpr unsigned WS_GEN = 0, WS_FLAG = 32768;
constexpr unsigned WS_KV = 196608;
constexpr unsigned WS_Q = WS_KV + (4u << 20);
constexpr unsigned WS_O = WS_Q + (2u << 20);
constexpr unsigned WS_H = WS_O + (2u << 20);
constexpr unsigned WS_END = WS_H + 64u * 112u * 1024u;
constexpr int AUX_DEV = 16;
constexpr unsigned SENT = 0xFFFFFFFFu;
constexpr int U = 8192;
constexpr int LDS_RING = 8 * U, LDS_QX = 17 * U;

#define MFMA(a, b, c) __builtin_amdgcn_mfma_f32_32x32x16_bf16((a), (b), (c), 0, 0, 0)
#define PIN(x) asm volatile("" : "+v"(x))
#define MFMA16(a, b, c) __builtin_amdgcn_mfma_f32_16x16x32_bf16((a), (b), (c), 0, 0, 0)
#define BAR() asm volatile("s_waitcnt lgkmcnt(0)\n\ts_barrier" ::: "memory")

DI unsigned rfl(unsigned x) { return __builtin_amdgcn_readfirstlane(x); }
DI int lane_now() { int l; asm volatile("v_mbcnt_lo_u32_b32 %0, -1, 0\n\tv_mbcnt_hi_u32_b32 %0, -1, %0" : "=v"(l)); return l; }
#define RELOAD() asm volatile("" ::: "memory")
DI unsigned cvtpk(float lo, float hi) { f32x2 v = {lo, hi}; return __builtin_bit_cast(unsigned, __builtin_convertvector(v, bf16x2)); }
DI bf16x8 asbf(u32x4 v) { return __builtin_bit_cast(bf16x8, v); }
DI rsrc_t mkrsrc(const void* p, unsigned bytes) { return __builtin_amdgcn_make_buffer_rsrc((void*)p, 0, bytes, 0x00020000); }
DI void dma16(rsrc_t rs, unsigned voff, unsigned soff, unsigned ldsaddr) {
  asm volatile("s_mov_b32 m0, %0\n\ts_nop 3\n\tbuffer_load_dwordx4 %1, %2, %3 offen lds" ::"s"(ldsaddr), "v"(voff), "s"(rs), "s"(soff) : "memory");
}
DI lds_p LP(unsigned a) { return (lds_p)(uintptr_t)a; }
DI bf16x8 ldsr128(unsigned a, int off) { return *(LAS bf16x8*)(LP(a) + off); }
DI void ldsw128(unsigned a, int off, u32x4 v) { *(LAS u32x4*)(LP(a) + off) = v; }
DI u32x4 gld(rsrc_t rs, unsigned voff, unsigned soff) { return __builtin_amdgcn_raw_buffer_load_b128(rs, voff, soff, AUX_DEV); }
DI void gst(rsrc_t rs, u32x4 v, unsigned voff, unsigned soff) { __builtin_amdgcn_raw_buffer_store_b128(v, rs, voff, soff, AUX_DEV); }
DI void gst8(rsrc_t rs, u32x2 v, unsigned voff, unsigned soff) { __builtin_amdgcn_raw_buffer_store_b64(v, rs, voff, soff, AUX_DEV); }
DI u32x4 pack8(const f32x16& a, int s) { return (u32x4){cvtpk(a[8 * s], a[8 * s + 1]), cvtpk(a[8 * s + 2], a[8 * s + 3]), cvtpk(a[8 * s + 4], a[8 * s + 5]), cvtpk(a[8 * s + 6], a[8 * s + 7])}; }
DI float halfmax(float m) { auto t = __builtin_amdgcn_permlane32_swap(__float_as_uint(m), __float_as_uint(m), false, false); return __builtin_fmaxf(__uint_as_float(t[0]), __uint_as_float(t[1])); }
DI float max16(const f32x16& s) {
  float a = __builtin_fmaxf(s[0], s[1]);
#pragma unroll
  for (int i = 2; i < 16; i += 2) a = __builtin_fmaxf(__builtin_fmaxf(a, s[i]), s[i + 1]);
  return a;
}
constexpr unsigned long long SPIN_TICKS = 20000000ull;

DI unsigned cs_of(unsigned j) { return (unsigned)(0xDCBA9876F543E210ull >> (4 * j)) & 15; }

__global__ __launch_bounds__(512, 2) void fused_kernel(const void* __restrict__ xg, const void* __restrict__ wqkv, const void* __restrict__ wo,
                                                       const void* __restrict__ wg, const void* __restrict__ wu, const void* __restrict__ wd,
                                                       const void* __restrict__ gw, void* __restrict__ out, char* __restrict__ ws) {
  __shared__ __attribute__((aligned(16))) char smem[163840];
  const unsigned L0 = (unsigned)(uintptr_t)(lds_p)smem;
  if (L0 != 0) return;
  asm volatile("" ::"s"(xg), "s"(wqkv), "s"(wo), "s"(wg), "s"(wu), "s"(wd), "s"(gw), "s"(out), "s"(ws));
  const int lane = threadIdx.x & 63, w = rfl(threadIdx.x >> 6);
  const unsigned bid = blockIdx.x, xcd = bid & 7, idx = bid >> 3;

  const unsigned rb = (xcd & 1) * 8 + (idx & 7), cs = cs_of((xcd >> 1) * 4 + (idx >> 3));
  const bool isV = cs >= 14;
  const int dr = lane >> 3, dp = lane & 7, dsw = (dp ^ (((dr >> 1) & 1) << 2)) << 4;
  const rsrc_t rx = mkrsrc(xg, S_ * D_ * 2), rq = mkrsrc(wqkv, D_ * 3 * D_ * 2);
  unsigned vx = (8 * w + dr) * 896 + ((dp ^ (((w & 1) << 2) | (dr >> 1))) << 4);
  unsigned v2 = (8 * w + dr) * 2688 + dsw;
  const unsigned lc0 = isV ? 14 + (cs & 1) * 4 : cs;
  const unsigned sx = rb * 128 * 896, s2 = lc0 * 128, ldw = w * 1024;
  const unsigned rot0 = (idx & 7) + 2 * (idx >> 3), rot = rot0 >= 7 ? rot0 - 7 : rot0;
  dma16(rx, vx, sx + rot * 128, ldw); dma16(rx, vx, sx + 64 * 896 + rot * 128, ldw + U); dma16(rq, v2, s2 + rot * 64 * 2688, ldw + 2 * U);
  const rsrc_t r3 = mkrsrc(isV ? wqkv : wg, isV ? D_ * 3 * D_ * 2 : D_ * F_ * 2), r4 = mkrsrc(isV ? wqkv : wu, isV ? D_ * 3 * D_ * 2 : D_ * F_ * 2);
  unsigned v3 = (8 * w + dr) * (isV ? 2688 : 3584) + dsw, v4 = isV ? v3 + 128 : v3;
  const unsigned s3 = isV ? s2 + 128 : cs * 256, st34 = isV ? 64 * 2688 : 64 * 3584;
  dma16(r3, v3, s3 + rot * st34, ldw + 3 * U);
  const rsrc_t rw = mkrsrc(ws, WS_END), rd = mkrsrc(wd, F_ * D_ * 2), ro = mkrsrc(wo, D_ * D_ * 2);
  f32x16 d00, d01, d10, d11, wo0, wo1, wo2, wo3;
  unsigned l16, hv, h0, h1, gr, okw, redo, hc, ob, mring;
  unsigned long long ep, op;
  float gq;
  { unsigned la = lane; PIN(la);
    const unsigned rt = w & 3, half = w >> 2;
    hv = dr * 896 + dsw;
    const unsigned r = la & 31, blk = (la >> 4) & 1, q4 = (la >> 2) & 3, p4 = la & 3, swq = (q4 >> 1) << 2;
    const unsigned hh = la >> 5; h0 = hh;
    const unsigned base = (8 * hh + q4) * 128 + (((4 * half + 2 * blk) ^ swq) << 4), nat = base + p4 * 8, aqk = base + (p4 & 1) * 16 + (p4 >> 1) * 8;
    unsigned A0 = 2 * U + (isV ? nat : aqk), A1 = 3 * U + nat;
    unsigned xb0 = (rt >> 1) * U + ((rt & 1) * 32 + r) * 128 + ((hh ^ ((r >> 1) & 7)) << 4), xb1 = xb0 ^ 32, xb2 = xb0 ^ 64, xb3 = xb0 ^ 96;
    unsigned W0 = LDS_RING + w * 2048 + (4 * hh + q4) * 128 + (((2 * blk) ^ swq) << 4) + p4 * 8;
    l16 = la * 16;
    const unsigned ty = ((0x7Fu >> cs) & 1) | ((0x18000u >> cs) & 2) | ((half ^ cs ^ 1) & 1) << 2 | ((0x8000u >> cs) & ~half & 1) << 3 | ((bid >> 3) & 16);
    asm volatile(
    "s_add_u32 s90, %[rot], 1\n\t"
    "s_cmp_eq_u32 s90, 7\n\t"
    "s_cselect_b32 s90, 0, s90\n\t"
    "s_lshl_b32 s86, s90, 7\n\t"
    "s_mul_i32 s87, s90, 172032\n\t"
    "s_mul_i32 s88, s90, %[st34]\n\t"
    "s_add_u32 s86, s86, %[sx]\n\t"
    "s_add_u32 s87, s87, %[s2]\n\t"
    "s_add_u32 s88, s88, %[s3]\n\t"
    "s_add_u32 s46, %[sx], 896\n\t"
    "s_add_u32 s47, %[s2], 1204224\n\t"
    "s_mul_i32 s48, %[st34], 7\n\t"
    "s_add_u32 s48, s48, %[s3]\n\t"
    "s_mov_b32 s89, %[s3]\n\t"
    "v_add_u32 v241, 57344, v216\n\t"
    "buffer_load_dwordx2 v[120:121], off, %[rw], 0 sc1\n\t"
    "s_and_b32 s81, %[bid], 7\n\t"
    "s_lshr_b32 s82, %[bid], 7\n\t"
    "s_lshl1_add_u32 s50, s81, s82\n\t"
    "s_bfe_u32 s51, %[bid], 0x40003\n\t"
    "s_lshr_b32 s82, %[bid], 3\n\t"
    "s_add_u32 s83, s82, -28\n\t"
    "s_cmp_lt_u32 s82, 28\n\t"
    "s_cselect_b32 s82, s82, s83\n\t"
    "s_mul_i32 s83, s82, 37\n\t"
    "s_lshr_b32 s83, s83, 8\n\t"
    "s_lshl2_add_u32 s52, s81, s83\n\t"
    "s_mul_i32 s83, s83, 7\n\t"
    "s_sub_u32 s53, s82, s83\n\t"
    "s_lshl_b32 s53, s53, 7\n\t"
    "s_and_b32 s81, %[wv], 3\n\t"
    "s_lshl2_add_u32 s91, %[rb], s81\n\t"
    "s_lshl4_add_u32 s71, %[rb], %[cs]\n\t"
    "s_lshl_b32 s71, s71, 7\n\t"
    "s_add_u32 s71, s71, 32768\n\t"
    "s_sub_u32 s74, 2176, %[s3]\n\t"
    "s_bitcmp1_b32 %[ty], 1\n\t"
    "s_cselect_b32 s74, s74, 128\n\t"
    "s_bitcmp1_b32 %[ty], 1\n\t"
    "s_cbranch_scc1 Lq_%=\n\t"
    "s_lshr_b32 s72, %[s2], 4\n\t"
    "s_add_u32 s81, s72, -56\n\t"
    "s_bitcmp1_b32 %[ty], 0\n\t"
    "s_cselect_b32 s72, s72, s81\n\t"
    "s_cselect_b32 s73, 0x3e8b97f5, 1.0\n\t"
    "s_and_b32 s81, %[wv], 4\n\t"
    "s_add_u32 s72, s72, s81\n\t"
    "s_lshl_b32 s81, s91, 11\n\t"
    "s_add_u32 s82, s81, 4390912\n\t"
    "s_lshl1_add_u32 s81, s81, 196608\n\t"
    "s_bitcmp1_b32 %[ty], 0\n\t"
    "s_cselect_b32 s81, s82, s81\n\t"
    "s_cselect_b32 s66, 18, 19\n\t"
    "s_lshl_b32 s67, 1, s66\n\t"
    "s_lshr_b32 s67, s67, 1\n\t"
    "s_add_u32 s68, s67, -1536\n\t"
    "v_and_b32 v243, 0x1f0, v252\n\t"
    "v_add_u32 v243, s81, v243\n\t"
    "v_mov_b32 v164, 0x3f803f80\n\t"
    "v_mov_b32 v165, 0x3f80\n\t"
    "s_add_u32 s81, s72, 0\n\t"
    "v_add_u32 v32, s81, v244\n\t"
    "v_mul_u32_u24 v33, 9363, v32\n\t"
    "v_lshrrev_b32 v33, 16, v33\n\t"
    "v_mad_i32_i24 v32, v33, -7, v32\n\t"
    "v_lshl_add_u32 v33, v33, s66, v243\n\t"
    "v_lshl_add_u32 v236, v32, 9, v33\n\t"
    "v_cmp_lt_u32 s[92:93], 3, v32\n\t"
    "v_cmp_eq_u32 s[78:79], 3, v32\n\t"
    "v_add_u32 v34, s68, v236\n\t"
    "v_add_u32 v33, s67, v33\n\t"
    "v_cndmask_b32 v236, v236, v34, s[92:93]\n\t"
    "v_cndmask_b32 v238, 2.0, v33, s[78:79]\n\t"
    "s_add_u32 s81, s72, 2\n\t"
    "v_add_u32 v32, s81, v244\n\t"
    "v_mul_u32_u24 v33, 9363, v32\n\t"
    "v_lshrrev_b32 v33, 16, v33\n\t"
    "v_mad_i32_i24 v32, v33, -7, v32\n\t"
    "v_lshl_add_u32 v33, v33, s66, v243\n\t"
    "v_lshl_add_u32 v237, v32, 9, v33\n\t"
    "v_cmp_lt_u32 s[92:93], 3, v32\n\t"
    "v_cmp_eq_u32 s[54:55], 3, v32\n\t"
    "v_add_u32 v34, s68, v237\n\t"
    "v_add_u32 v33, s67, v33\n\t"
    "v_cndmask_b32 v237, v237, v34, s[92:93]\n\t"
    "v_cndmask_b32 v239, 2.0, v33, s[54:55]\n\t"
    "s_lshl_b32 s81, s50, 18\n\t"
    "s_lshl_b32 s82, %[wv], 15\n\t"
    "s_add_u32 s81, s81, s82\n\t"
    "s_add_u32 s56, s81, 196608\n\t"
    "s_lshl1_add_u32 s70, %[ldw], 65536\n\t"
    "s_lshl3_add_u32 s81, s52, %[wv]\n\t"
    "s_mul_i32 s81, s81, 28672\n\t"
    "s_add_u32 s57, s81, 8585216\n\t"
    "s_and_b32 s81, %[wv], 4\n\t"
    "s_add_u32 s81, s81, %[wv]\n\t"
    "s_mul_i32 s81, s81, 14336\n\t"
    "s_add_u32 s59, s81, s53\n\t"
    "s_mul_i32 s81, %[wv], 7168\n\t"
    "s_add_u32 s60, s81, s53\n\t"
    "s_mul_i32 s81, s52, 57344\n\t"
    "s_add_u32 s81, s81, s60\n\t"
    "s_mov_b64 s[76:77], %[out]\n\t"
    "s_add_u32 s76, s76, s81\n\t"
    "s_addc_u32 s77, s77, 0\n\t"
    "s_lshl4_add_u32 s81, s50, s51\n\t"
    "s_lshl2_add_u32 s81, s81, %[wv]\n\t"
    "s_lshl_b32 s81, s81, 11\n\t"
    "s_add_u32 s81, s81, 4390912\n\t"
    "s_cmp_lt_u32 %[wv], 4\n\t"
    "s_cselect_b32 s61, s81, 2.0\n\t"
    "s_lshl_b32 s81, s91, 7\n\t"
    "s_add_u32 s62, s81, 16384\n\t"
    "s_and_b32 s82, %[wv], 1\n\t"
    "s_lshl1_add_u32 s83, s52, s82\n\t"
    "s_lshl_b32 s81, s83, 7\n\t"
    "s_add_u32 s63, s81, 16384\n\t"
    "s_lshr_b32 s82, %[wv], 1\n\t"
    "s_lshl2_add_u32 s81, s83, s82\n\t"
    "s_lshl_b32 s81, s81, 13\n\t"
    "s_add_u32 s69, s81, 6488064\n\t"
    "s_lshl3_add_u32 s81, s52, %[wv]\n\t"
    "s_lshl_b32 s81, s81, 5\n\t"
    "s_add_u32 s64, s81, 16384\n\t"
    "s_lshl3_add_u32 s81, s51, %[wv]\n\t"
    "s_lshl_b32 s81, s81, 6\n\t"
    "s_add_u32 s75, s81, 16384\n\t"
    "s_lshr_b32 s81, s91, 1\n\t"
    "s_and_b32 s82, %[cs], 1\n\t"
    "s_lshl1_add_u32 s81, s81, s82\n\t"
    "s_lshr_b32 s82, %[wv], 2\n\t"
    "s_lshl1_add_u32 s81, s81, s82\n\t"
    "s_mul_i32 s81, s81, 28\n\t"
    "s_lshr_b32 s82, %[cs], 1\n\t"
    "s_add_u32 s81, s81, s82\n\t"
    "s_and_b32 s82, s91, 1\n\t"
    "s_lshl1_add_u32 s81, s81, s82\n\t"
    "s_lshl_b32 s81, s81, 10\n\t"
    "s_add_u32 s81, s81, 8585216\n\t"
    "s_bitcmp1_b32 %[ty], 1\n\t"
    "s_cselect_b32 s65, 2.0, s81\n\t"
    "s_xor_b32 s58, s65, 1024\n\t"
    "s_add_u32 s58, s58, 28672\n\t"
    "s_lshr_b32 s82, %[wv], 1\n\t"
    "s_lshl2_add_u32 s81, s51, s82\n\t"
    "s_lshl4_add_u32 s81, s81, s50\n\t"
    "s_and_b32 s83, %[wv], 1\n\t"
    "s_lshl3_add_u32 s81, s81, s83\n\t"
    "s_lshl_b32 s81, s81, 8\n\t"
    "s_add_u32 s66, s81, 6488064\n\t"
    "s_lshl4_add_u32 s81, s51, s50\n\t"
    "s_lshl_b32 s81, s81, 7\n\t"
    "s_add_u32 s67, s81, 131072\n\t"
    "s_lshr_b32 s83, s52, 1\n\t"
    "s_lshl2_add_u32 s81, s83, s82\n\t"
    "s_lshl_b32 s81, s81, 9\n\t"
    "s_add_u32 s68, s81, 131072\n\t"
    "s_cmp_lt_u32 %[bid], 224\n\t"
    "s_cselect_b32 s57, s57, 2.0\n\t"
    "s_cselect_b32 s59, s59, 2.0\n\t"
    "s_cselect_b32 s60, s60, 2.0\n\t"
    "v_xor_b32 v250, 64, v249\n\t"
    "v_add_u32 v246, 139264, v252\n\t"
    "s_lshr_b32 s81, s52, 1\n\t"
    "s_lshl_b32 s81, s81, 11\n\t"
    "s_add_u32 s81, s81, 32768\n\t"
    "s_lshr_b32 s82, %[wv], 2\n\t"
    "s_lshl_b32 s82, s82, 7\n\t"
    "s_add_u32 s82, s82, s81\n\t"
    "s_add_u32 s83, s68, -32768\n\t"
    "v_lshl_add_u32 v251, v252, 4, s82\n\t"
    "v_and_b32 v32, 0x30, v252\n\t"
    "v_lshl_add_u32 v32, v32, 3, s83\n\t"
    "s_mov_b32 s92, 0xfffffe00\n\t"
    "s_mov_b32 s93, 0xffffffff\n\t"
    "v_cndmask_b32 v251, v251, v32, s[92:93]\n\t"
    "s_add_u32 s81, s81, 1920\n\t"
    "s_lshl_b32 s82, s51, 11\n\t"
    "s_add_u32 s82, s82, 34688\n\t"
    "v_writelane_b32 v251, s81, 7\n\t"
    "v_writelane_b32 v251, s82, 8\n\t"
    "s_mul_i32 s81, s50, 28\n\t"
    "s_add_u32 s83, s81, 27\n\t"
    "s_lshr_b32 s81, s81, 6\n\t"
    "s_lshr_b32 s83, s83, 6\n\t"
    "s_sub_u32 s83, s83, s81\n\t"
    "s_lshl_b32 s83, s83, 3\n\t"
    "s_lshl_b32 s81, s81, 7\n\t"
    "s_add_u32 s82, s82, s81\n\t"
    "s_add_u32 s82, s82, -1920\n\t"
    "v_and_b32 v32, 16, v252\n\t"
    "v_and_b32 v34, 64, v252\n\t"
    "v_lshlrev_b32 v34, 5, v34\n\t"
    "v_lshl_add_u32 v33, v32, 3, v34\n\t"
    "v_mul_u32_u24 v32, s83, v32\n\t"
    "s_lshl_b32 s83, %[ldw], 2\n\t"
    "s_add_u32 s81, s81, s83\n\t"
    "s_add_u32 s81, s81, 33664\n\t"
    "s_add_u32 s83, s83, 67328\n\t"
    "v_add_u32 v33, s83, v33\n\t"
    "v_add3_u32 v34, v34, v32, s81\n\t"
    "v_add_u32 v32, s82, v32\n\t"
    "s_mov_b32 s92, 0xcccccccc\n\t"
    "s_mov_b32 s93, 0xcccccccc\n\t"
    "v_cndmask_b32 v32, v34, v32, s[92:93]\n\t"
    "s_mov_b32 s92, 0x0\n\t"
    "s_mov_b32 s93, 0xffffffff\n\t"
    "v_cndmask_b32 v245, v32, v33, s[92:93]\n\t"
    "s_add_u32 s50, s56, 4096\n\t"
    "s_add_u32 s51, s56, 8192\n\t"
    "s_add_u32 s52, s56, 12288\n\t"
    "s_add_u32 s53, s56, 16384\n\t"
    "s_add_u32 m0, %[ldw], 32768\n\t"
    "s_nop 0\n\t"
    "buffer_load_dwordx4 v216, %[rx], s86 offen lds\n\t"
    "s_add_u32 m0, %[ldw], 40960\n\t"
    "s_nop 0\n\t"
    "buffer_load_dwordx4 v241, %[rx], s86 offen lds\n\t"
    "s_add_u32 s86, s86, 128\n\t"
    "s_cmp_eq_u32 s86, s46\n\t"
    "s_cselect_b32 s86, %[sx], s86\n\t"
    "s_add_u32 m0, %[ldw], 49152\n\t"
    "s_nop 0\n\t"
    "buffer_load_dwordx4 v217, %[rq], s87 offen lds\n\t"
    "s_add_u32 s87, s87, 172032\n\t"
    "s_cmp_eq_u32 s87, s47\n\t"
    "s_cselect_b32 s87, %[s2], s87\n\t"
    "s_add_u32 m0, %[ldw], 57344\n\t"
    "s_nop 0\n\t"
    "buffer_load_dwordx4 v218, %[r3], s88 offen lds\n\t"
    "s_add_u32 s88, s88, %[st34]\n\t"
    "s_cmp_eq_u32 s88, s48\n\t"
    "s_cselect_b32 s88, s89, s88\n\t"
    "s_add_u32 m0, %[ldw], 65536\n\t"
    "s_nop 0\n\t"
    "buffer_load_dwordx4 v216, %[rx], s86 offen lds\n\t"
    "s_add_u32 m0, %[ldw], 73728\n\t"
    "s_nop 0\n\t"
    "buffer_load_dwordx4 v241, %[rx], s86 offen lds\n\t"
    "s_add_u32 s86, s86, 128\n\t"
    "s_cmp_eq_u32 s86, s46\n\t"
    "s_cselect_b32 s86, %[sx], s86\n\t"
    "s_waitcnt vmcnt(7)\n\t"
    "s_waitcnt lgkmcnt(0)\n\t"
    "s_barrier\n\t"
    "ds_read_b128 v[168:171], v232\n\t"
    "ds_read_b64_tr_b16 v[136:137], v220\n\t"
    "ds_read_b64_tr_b16 v[138:139], v220 offset:512\n\t"
    "ds_read_b64_tr_b16 v[140:141], v221\n\t"
    "ds_read_b64_tr_b16 v[142:143], v221 offset:512\n\t"
    "ds_read_b128 v[172:175], v233\n\t"
    "ds_read_b64_tr_b16 v[144:145], v220 offset:2048\n\t"
    "ds_read_b64_tr_b16 v[146:147], v220 offset:2560\n\t"
    "ds_read_b64_tr_b16 v[148:149], v221 offset:2048\n\t"
    "ds_read_b64_tr_b16 v[150:151], v221 offset:2560\n\t"
    "s_waitcnt lgkmcnt(5)\n\t"
    "v_mfma_f32_32x32x16_bf16 v[0:15], v[136:139], v[168:171], 0\n\t"
    "s_add_u32 m0, %[ldw], 81920\n\t"
    "ds_read_b128 v[224:227], v234\n\t"
    "ds_read_b64_tr_b16 v[152:153], v220 offset:4096\n\t"
    "ds_read_b64_tr_b16 v[154:155], v220 offset:4608\n\t"
    "buffer_load_dwordx4 v217, %[rq], s87 offen lds\n\t"
    "s_add_u32 s87, s87, 172032\n\t"
    "s_cmp_eq_u32 s87, s47\n\t"
    "s_cselect_b32 s87, %[s2], s87\n\t"
    "v_mfma_f32_32x32x16_bf16 v[16:31], v[140:143], v[168:171], 0\n\t"
    "s_add_u32 m0, %[ldw], 90112\n\t"
    "ds_read_b64_tr_b16 v[156:157], v221 offset:4096\n\t"
    "ds_read_b64_tr_b16 v[158:159], v221 offset:4608\n\t"
    "buffer_load_dwordx4 v218, %[r3], s88 offen lds\n\t"
    "s_add_u32 s88, s88, %[st34]\n\t"
    "s_cmp_eq_u32 s88, s48\n\t"
    "s_cselect_b32 s88, s89, s88\n\t"
    "s_waitcnt lgkmcnt(5)\n\t"
    "v_mfma_f32_32x32x16_bf16 v[0:15], v[144:147], v[172:175], v[0:15]\n\t"
    "s_add_u32 m0, %[ldw], 98304\n\t"
    "ds_read_b128 v[228:231], v235\n\t"
    "ds_read_b64_tr_b16 v[136:137], v220 offset:6144\n\t"
    "ds_read_b64_tr_b16 v[138:139], v220 offset:6656\n\t"
    "buffer_load_dwordx4 v216, %[rx], s86 offen lds\n\t"
    "v_mfma_f32_32x32x16_bf16 v[16:31], v[148:151], v[172:175], v[16:31]\n\t"
    "s_add_u32 m0, %[ldw], 106496\n\t"
    "ds_read_b64_tr_b16 v[140:141], v221 offset:6144\n\t"
    "ds_read_b64_tr_b16 v[142:143], v221 offset:6656\n\t"
    "buffer_load_dwordx4 v241, %[rx], s86 offen lds\n\t"
    "s_add_u32 s86, s86, 128\n\t"
    "s_cmp_eq_u32 s86, s46\n\t"
    "s_cselect_b32 s86, %[sx], s86\n\t"
    "s_waitcnt vmcnt(6)\n\t"
    "s_waitcnt lgkmcnt(0)\n\t"
    "s_barrier\n\t"
    "v_mfma_f32_32x32x16_bf16 v[0:15], v[152:155], v[224:227], v[0:15]\n\t"
    "s_add_u32 m0, %[ldw], 114688\n\t"
    "v_add_u32 v232, 32768, v232\n\t"
    "ds_read_b128 v[184:187], v232\n\t"
    "v_add_u32 v220, 32768, v220\n\t"
    "ds_read_b64_tr_b16 v[144:145], v220\n\t"
    "ds_read_b64_tr_b16 v[146:147], v220 offset:512\n\t"
    "buffer_load_dwordx4 v217, %[rq], s87 offen lds\n\t"
    "s_add_u32 s87, s87, 172032\n\t"
    "s_cmp_eq_u32 s87, s47\n\t"
    "s_cselect_b32 s87, %[s2], s87\n\t"
    "v_mfma_f32_32x32x16_bf16 v[16:31], v[156:159], v[224:227], v[16:31]\n\t"
    "s_add_u32 m0, %[ldw], 122880\n\t"
    "v_add_u32 v221, 32768, v221\n\t"
    "ds_read_b64_tr_b16 v[148:149], v221\n\t"
    "ds_read_b64_tr_b16 v[150:151], v221 offset:512\n\t"
    "buffer_load_dwordx4 v218, %[r3], s88 offen lds\n\t"
    "s_add_u32 s88, s88, %[st34]\n\t"
    "s_cmp_eq_u32 s88, s48\n\t"
    "s_cselect_b32 s88, s89, s88\n\t"
    "v_mfma_f32_32x32x16_bf16 v[0:15], v[136:139], v[228:231], v[0:15]\n\t"
    "s_add_u32 m0, %[ldw], 131072\n\t"
    "v_add_u32 v233, 32768, v233\n\t"
    "ds_read_b128 v[188:191], v233\n\t"
    "ds_read_b64_tr_b16 v[152:153], v220 offset:2048\n\t"
    "ds_read_b64_tr_b16 v[154:155], v220 offset:2560\n\t"
    "buffer_load_dwordx4 v216, %[rx], s86 offen lds\n\t"
    "v_mfma_f32_32x32x16_bf16 v[16:31], v[140:143], v[228:231], v[16:31]\n\t"
    "s_add_u32 m0, %[ldw], 139264\n\t"
    "ds_read_b64_tr_b16 v[156:157], v221 offset:2048\n\t"
    "ds_read_b64_tr_b16 v[158:159], v221 offset:2560\n\t"
    "buffer_load_dwordx4 v241, %[rx], s86 offen lds\n\t"
    "s_add_u32 s86, s86, 128\n\t"
    "s_cmp_eq_u32 s86, s46\n\t"
    "s_cselect_b32 s86, %[sx], s86\n\t"
    "s_waitcnt lgkmcnt(5)\n\t"
    "v_mfma_f32_32x32x16_bf16 v[0:15], v[144:147], v[184:187], v[0:15]\n\t"
    "s_add_u32 m0, %[ldw], 147456\n\t"
    "v_add_u32 v234, 32768, v234\n\t"
    "ds_read_b128 v[176:179], v234\n\t"
    "ds_read_b64_tr_b16 v[136:137], v220 offset:4096\n\t"
    "ds_read_b64_tr_b16 v[138:139], v220 offset:4608\n\t"
    "buffer_load_dwordx4 v217, %[rq], s87 offen lds\n\t"
    "s_add_u32 s87, s87, 172032\n\t"
    "s_cmp_eq_u32 s87, s47\n\t"
    "s_cselect_b32 s87, %[s2], s87\n\t"
    "v_mfma_f32_32x32x16_bf16 v[16:31], v[148:151], v[184:187], v[16:31]\n\t"
    "s_add_u32 m0, %[ldw], 155648\n\t"
    "ds_read_b64_tr_b16 v[140:141], v221 offset:4096\n\t"
    "ds_read_b64_tr_b16 v[142:143], v221 offset:4608\n\t"
    "buffer_load_dwordx4 v218, %[r3], s88 offen lds\n\t"
    "s_add_u32 s88, s88, %[st34]\n\t"
    "s_cmp_eq_u32 s88, s48\n\t"
    "s_cselect_b32 s88, s89, s88\n\t"
    "s_waitcnt lgkmcnt(5)\n\t"
    "v_mfma_f32_32x32x16_bf16 v[0:15], v[152:155], v[188:191], v[0:15]\n\t"
    "v_add_u32 v235, 32768, v235\n\t"
    "ds_read_b128 v[180:183], v235\n\t"
    "ds_read_b64_tr_b16 v[144:145], v220 offset:6144\n\t"
    "ds_read_b64_tr_b16 v[146:147], v220 offset:6656\n\t"
    "v_mfma_f32_32x32x16_bf16 v[16:31], v[156:159], v[188:191], v[16:31]\n\t"
    "s_add_u32 m0, %[ldw], 0\n\t"
    "ds_read_b64_tr_b16 v[148:149], v221 offset:6144\n\t"
    "ds_read_b64_tr_b16 v[150:151], v221 offset:6656\n\t"
    "buffer_load_dwordx4 v216, %[rx], s86 offen lds\n\t"
    "s_waitcnt vmcnt(9)\n\t"
    "s_waitcnt lgkmcnt(0)\n\t"
    "s_barrier\n\t"
    "v_mfma_f32_32x32x16_bf16 v[0:15], v[136:139], v[176:179], v[0:15]\n\t"
    "v_add_u32 v232, 32768, v232\n\t"
    "ds_read_b128 v[192:195], v232\n\t"
    "v_add_u32 v220, 32768, v220\n\t"
    "ds_read_b64_tr_b16 v[152:153], v220\n\t"
    "ds_read_b64_tr_b16 v[154:155], v220 offset:512\n\t"
    "v_mfma_f32_32x32x16_bf16 v[16:31], v[140:143], v[176:179], v[16:31]\n\t"
    "s_add_u32 m0, %[ldw], 8192\n\t"
    "v_add_u32 v221, 32768, v221\n\t"
    "ds_read_b64_tr_b16 v[156:157], v221\n\t"
    "ds_read_b64_tr_b16 v[158:159], v221 offset:512\n\t"
    "buffer_load_dwordx4 v241, %[rx], s86 offen lds\n\t"
    "s_add_u32 s86, s86, 128\n\t"
    "s_cmp_eq_u32 s86, s46\n\t"
    "s_cselect_b32 s86, %[sx], s86\n\t"
    "v_mfma_f32_32x32x16_bf16 v[0:15], v[144:147], v[180:183], v[0:15]\n\t"
    "v_add_u32 v233, 32768, v233\n\t"
    "ds_read_b128 v[196:199], v233\n\t"
    "ds_read_b64_tr_b16 v[136:137], v220 offset:2048\n\t"
    "ds_read_b64_tr_b16 v[138:139], v220 offset:2560\n\t"
    "v_mfma_f32_32x32x16_bf16 v[16:31], v[148:151], v[180:183], v[16:31]\n\t"
    "s_add_u32 m0, %[ldw], 16384\n\t"
    "ds_read_b64_tr_b16 v[140:141], v221 offset:2048\n\t"
    "ds_read_b64_tr_b16 v[142:143], v221 offset:2560\n\t"
    "buffer_load_dwordx4 v217, %[rq], s87 offen lds\n\t"
    "s_add_u32 s87, s87, 172032\n\t"
    "s_cmp_eq_u32 s87, s47\n\t"
    "s_cselect_b32 s87, %[s2], s87\n\t"
    "s_waitcnt lgkmcnt(5)\n\t"
    "v_mfma_f32_32x32x16_bf16 v[0:15], v[152:155], v[192:195], v[0:15]\n\t"
    "v_add_u32 v234, 32768, v234\n\t"
    "ds_read_b128 v[200:203], v234\n\t"
    "ds_read_b64_tr_b16 v[144:145], v220 offset:4096\n\t"
    "ds_read_b64_tr_b16 v[146:147], v220 offset:4608\n\t"
    "v_mfma_f32_32x32x16_bf16 v[16:31], v[156:159], v[192:195], v[16:31]\n\t"
    "s_add_u32 m0, %[ldw], 24576\n\t"
    "ds_read_b64_tr_b16 v[148:149], v221 offset:4096\n\t"
    "ds_read_b64_tr_b16 v[150:151], v221 offset:4608\n\t"
    "buffer_load_dwordx4 v218, %[r3], s88 offen lds\n\t"
    "s_add_u32 s88, s88, %[st34]\n\t"
    "s_cmp_eq_u32 s88, s48\n\t"
    "s_cselect_b32 s88, s89, s88\n\t"
    "s_waitcnt lgkmcnt(5)\n\t"
    "v_mfma_f32_32x32x16_bf16 v[0:15], v[136:139], v[196:199], v[0:15]\n\t"
    "v_add_u32 v235, 32768, v235\n\t"
    "ds_read_b128 v[204:207], v235\n\t"
    "ds_read_b64_tr_b16 v[152:153], v220 offset:6144\n\t"
    "ds_read_b64_tr_b16 v[154:155], v220 offset:6656\n\t"
    "v_mfma_f32_32x32x16_bf16 v[16:31], v[140:143], v[196:199], v[16:31]\n\t"
    "s_add_u32 m0, %[ldw], 32768\n\t"
    "ds_read_b64_tr_b16 v[156:157], v221 offset:6144\n\t"
    "ds_read_b64_tr_b16 v[158:159], v221 offset:6656\n\t"
    "buffer_load_dwordx4 v216, %[rx], s86 offen lds\n\t"
    "s_waitcnt vmcnt(9)\n\t"
    "s_waitcnt lgkmcnt(0)\n\t"
    "s_barrier\n\t"
    "v_mfma_f32_32x32x16_bf16 v[0:15], v[144:147], v[200:203], v[0:15]\n\t"
    "v_add_u32 v232, 32768, v232\n\t"
    "ds_read_b128 v[208:211], v232\n\t"
    "v_add_u32 v220, 32768, v220\n\t"
    "ds_read_b64_tr_b16 v[136:137], v220\n\t"
    "ds_read_b64_tr_b16 v[138:139], v220 offset:512\n\t"
    "v_mfma_f32_32x32x16_bf16 v[16:31], v[148:151], v[200:203], v[16:31]\n\t"
    "s_add_u32 m0, %[ldw], 40960\n\t"
    "v_add_u32 v221, 32768, v221\n\t"
    "ds_read_b64_tr_b16 v[140:141], v221\n\t"
    "ds_read_b64_tr_b16 v[142:143], v221 offset:512\n\t"
    "buffer_load_dwordx4 v241, %[rx], s86 offen lds\n\t"
    "s_add_u32 s86, s86, 128\n\t"
    "s_cmp_eq_u32 s86, s46\n\t"
    "s_cselect_b32 s86, %[sx], s86\n\t"
    "v_mfma_f32_32x32x16_bf16 v[0:15], v[152:155], v[204:207], v[0:15]\n\t"
    "s_add_u32 m0, %[ldw], 49152\n\t"
    "v_add_u32 v233, 32768, v233\n\t"
    "ds_read_b128 v[212:215], v233\n\t"
    "ds_read_b64_tr_b16 v[144:145], v220 offset:2048\n\t"
    "ds_read_b64_tr_b16 v[146:147], v220 offset:2560\n\t"
    "buffer_load_dwordx4 v217, %[rq], s87 offen lds\n\t"
    "s_add_u32 s87, s87, 172032\n\t"
    "s_cmp_eq_u32 s87, s47\n\t"
    "s_cselect_b32 s87, %[s2], s87\n\t"
    "v_mfma_f32_32x32x16_bf16 v[16:31], v[156:159], v[204:207], v[16:31]\n\t"
    "s_add_u32 m0, %[ldw], 57344\n\t"
    "ds_read_b64_tr_b16 v[148:149], v221 offset:2048\n\t"
    "ds_read_b64_tr_b16 v[150:151], v221 offset:2560\n\t"
    "buffer_load_dwordx4 v218, %[r3], s88 offen lds\n\t"
    "s_add_u32 s88, s88, %[st34]\n\t"
    "s_cmp_eq_u32 s88, s48\n\t"
    "s_cselect_b32 s88, s89, s88\n\t"
    "s_waitcnt lgkmcnt(5)\n\t"
    "v_mfma_f32_32x32x16_bf16 v[0:15], v[136:139], v[208:211], v[0:15]\n\t"
    "v_add_u32 v234, 32768, v234\n\t"
    "ds_read_b128 v[64:67], v234\n\t"
    "ds_read_b64_tr_b16 v[152:153], v220 offset:4096\n\t"
    "ds_read_b64_tr_b16 v[154:155], v220 offset:4608\n\t"
    "v_mfma_f32_32x32x16_bf16 v[16:31], v[140:143], v[208:211], v[16:31]\n\t"
    "s_add_u32 m0, %[ldw], 65536\n\t"
    "ds_read_b64_tr_b16 v[156:157], v221 offset:4096\n\t"
    "ds_read_b64_tr_b16 v[158:159], v221 offset:4608\n\t"
    "buffer_load_dwordx4 v219, %[r4], s88 offen lds\n\t"
    "s_add_u32 s88, s88, %[st34]\n\t"
    "s_cmp_eq_u32 s88, s48\n\t"
    "s_cselect_b32 s88, s89, s88\n\t"
    "s_waitcnt lgkmcnt(5)\n\t"
    "v_mfma_f32_32x32x16_bf16 v[0:15], v[144:147], v[212:215], v[0:15]\n\t"
    "v_add_u32 v235, 32768, v235\n\t"
    "ds_read_b128 v[68:71], v235\n\t"
    "ds_read_b64_tr_b16 v[136:137], v220 offset:6144\n\t"
    "ds_read_b64_tr_b16 v[138:139], v220 offset:6656\n\t"
    "v_mfma_f32_32x32x16_bf16 v[16:31], v[148:151], v[212:215], v[16:31]\n\t"
    "s_add_u32 m0, %[ldw], 73728\n\t"
    "ds_read_b64_tr_b16 v[140:141], v221 offset:6144\n\t"
    "ds_read_b64_tr_b16 v[142:143], v221 offset:6656\n\t"
    "buffer_load_dwordx4 v219, %[r4], s88 offen lds\n\t"
    "s_add_u32 s88, s88, %[st34]\n\t"
    "s_cmp_eq_u32 s88, s48\n\t"
    "s_cselect_b32 s88, s89, s88\n\t"
    "s_waitcnt vmcnt(10)\n\t"
    "s_waitcnt lgkmcnt(0)\n\t"
    "s_barrier\n\t"
    "v_mfma_f32_32x32x16_bf16 v[0:15], v[152:155], v[64:67], v[0:15]\n\t"
    "v_add_u32 v232, 32768, v232\n\t"
    "ds_read_b128 v[72:75], v232\n\t"
    "v_add_u32 v220, 32768, v220\n\t"
    "ds_read_b64_tr_b16 v[144:145], v220\n\t"
    "ds_read_b64_tr_b16 v[146:147], v220 offset:512\n\t"
    "v_mfma_f32_32x32x16_bf16 v[16:31], v[156:159], v[64:67], v[16:31]\n\t"
    "s_add_u32 m0, %[ldw], 81920\n\t"
    "v_add_u32 v221, 32768, v221\n\t"
    "ds_read_b64_tr_b16 v[148:149], v221\n\t"
    "ds_read_b64_tr_b16 v[150:151], v221 offset:512\n\t"
    "buffer_load_dwordx4 v219, %[r4], s88 offen lds\n\t"
    "s_add_u32 s88, s88, %[st34]\n\t"
    "s_cmp_eq_u32 s88, s48\n\t"
    "s_cselect_b32 s88, s89, s88\n\t"
    "v_mfma_f32_32x32x16_bf16 v[0:15], v[136:139], v[68:71], v[0:15]\n\t"
    "v_add_u32 v233, 32768, v233\n\t"
    "ds_read_b128 v[76:79], v233\n\t"
    "ds_read_b64_tr_b16 v[152:153], v220 offset:2048\n\t"
    "ds_read_b64_tr_b16 v[154:155], v220 offset:2560\n\t"
    "v_mfma_f32_32x32x16_bf16 v[16:31], v[140:143], v[68:71], v[16:31]\n\t"
    "s_add_u32 m0, %[ldw], 90112\n\t"
    "ds_read_b64_tr_b16 v[156:157], v221 offset:2048\n\t"
    "ds_read_b64_tr_b16 v[158:159], v221 offset:2560\n\t"
    "buffer_load_dwordx4 v219, %[r4], s88 offen lds\n\t"
    "s_add_u32 s88, s88, %[st34]\n\t"
    "s_cmp_eq_u32 s88, s48\n\t"
    "s_cselect_b32 s88, s89, s88\n\t"
    "s_waitcnt lgkmcnt(5)\n\t"
    "v_mfma_f32_32x32x16_bf16 v[0:15], v[144:147], v[72:75], v[0:15]\n\t"
    "v_add_u32 v234, 32768, v234\n\t"
    "ds_read_b128 v[80:83], v234\n\t"
    "ds_read_b64_tr_b16 v[136:137], v220 offset:4096\n\t"
    "ds_read_b64_tr_b16 v[138:139], v220 offset:4608\n\t"
    "v_mfma_f32_32x32x16_bf16 v[16:31], v[148:151], v[72:75], v[16:31]\n\t"
    "s_add_u32 m0, %[ldw], 98304\n\t"
    "ds_read_b64_tr_b16 v[140:141], v221 offset:4096\n\t"
    "ds_read_b64_tr_b16 v[142:143], v221 offset:4608\n\t"
    "buffer_load_dwordx4 v219, %[r4], s88 offen lds\n\t"
    "s_add_u32 s88, s88, %[st34]\n\t"
    "s_cmp_eq_u32 s88, s48\n\t"
    "s_cselect_b32 s88, s89, s88\n\t"
    "s_waitcnt lgkmcnt(5)\n\t"
    "v_mfma_f32_32x32x16_bf16 v[0:15], v[152:155], v[76:79], v[0:15]\n\t"
    "v_add_u32 v235, 32768, v235\n\t"
    "ds_read_b128 v[84:87], v235\n\t"
    "ds_read_b64_tr_b16 v[144:145], v220 offset:6144\n\t"
    "ds_read_b64_tr_b16 v[146:147], v220 offset:6656\n\t"
    "v_mfma_f32_32x32x16_bf16 v[16:31], v[156:159], v[76:79], v[16:31]\n\t"
    "s_add_u32 m0, %[ldw], 106496\n\t"
    "ds_read_b64_tr_b16 v[148:149], v221 offset:6144\n\t"
    "ds_read_b64_tr_b16 v[150:151], v221 offset:6656\n\t"
    "buffer_load_dwordx4 v219, %[r4], s88 offen lds\n\t"
    "s_add_u32 s88, s88, %[st34]\n\t"
    "s_cmp_eq_u32 s88, s48\n\t"
    "s_cselect_b32 s88, s89, s88\n\t"
    "s_waitcnt vmcnt(10)\n\t"
    "s_waitcnt lgkmcnt(0)\n\t"
    "s_barrier\n\t"
    "v_mfma_f32_32x32x16_bf16 v[0:15], v[136:139], v[80:83], v[0:15]\n\t"
    "v_add_u32 v232, -131072, v232\n\t"
    "ds_read_b128 v[88:91], v232\n\t"
    "v_add_u32 v220, -131072, v220\n\t"
    "ds_read_b64_tr_b16 v[152:153], v220\n\t"
    "ds_read_b64_tr_b16 v[154:155], v220 offset:512\n\t"
    "v_mfma_f32_32x32x16_bf16 v[16:31], v[140:143], v[80:83], v[16:31]\n\t"
    "s_add_u32 m0, %[ldw], 114688\n\t"
    "v_add_u32 v221, -131072, v221\n\t"
    "ds_read_b64_tr_b16 v[156:157], v221\n\t"
    "ds_read_b64_tr_b16 v[158:159], v221 offset:512\n\t"
    "buffer_load_dwordx4 v219, %[r4], s88 offen lds\n\t"
    "s_add_u32 s88, s88, %[st34]\n\t"
    "s_cmp_eq_u32 s88, s48\n\t"
    "s_cselect_b32 s88, s89, s88\n\t"
    "s_add_u32 s88, s88, s74\n\t"
    "s_add_u32 s48, s48, s74\n\t"
    "s_add_u32 s89, s89, s74\n\t"
    "v_mfma_f32_32x32x16_bf16 v[0:15], v[144:147], v[84:87], v[0:15]\n\t"
    "v_add_u32 v233, -131072, v233\n\t"
    "ds_read_b128 v[92:95], v233\n\t"
    "ds_read_b64_tr_b16 v[136:137], v220 offset:2048\n\t"
    "ds_read_b64_tr_b16 v[138:139], v220 offset:2560\n\t"
    "v_mfma_f32_32x32x16_bf16 v[16:31], v[148:151], v[84:87], v[16:31]\n\t"
    "ds_read_b64_tr_b16 v[140:141], v221 offset:2048\n\t"
    "ds_read_b64_tr_b16 v[142:143], v221 offset:2560\n\t"
    "s_waitcnt lgkmcnt(5)\n\t"
    "v_mfma_f32_32x32x16_bf16 v[0:15], v[152:155], v[88:91], v[0:15]\n\t"
    "s_add_u32 m0, %[ldw], 122880\n\t"
    "v_add_u32 v234, -131072, v234\n\t"
    "ds_read_b128 v[96:99], v234\n\t"
    "ds_read_b64_tr_b16 v[144:145], v220 offset:4096\n\t"
    "ds_read_b64_tr_b16 v[146:147], v220 offset:4608\n\t"
    "buffer_load_dwordx4 v218, %[r3], s88 offen lds\n\t"
    "s_add_u32 s88, s88, %[st34]\n\t"
    "s_cmp_eq_u32 s88, s48\n\t"
    "s_cselect_b32 s88, s89, s88\n\t"
    "v_mfma_f32_32x32x16_bf16 v[16:31], v[156:159], v[88:91], v[16:31]\n\t"
    "ds_read_b64_tr_b16 v[148:149], v221 offset:4096\n\t"
    "ds_read_b64_tr_b16 v[150:151], v221 offset:4608\n\t"
    "s_waitcnt lgkmcnt(5)\n\t"
    "v_mfma_f32_32x32x16_bf16 v[0:15], v[136:139], v[92:95], v[0:15]\n\t"
    "s_add_u32 m0, %[ldw], 131072\n\t"
    "v_add_u32 v235, -131072, v235\n\t"
    "ds_read_b128 v[100:103], v235\n\t"
    "ds_read_b64_tr_b16 v[152:153], v220 offset:6144\n\t"
    "ds_read_b64_tr_b16 v[154:155], v220 offset:6656\n\t"
    "buffer_load_dwordx4 v218, %[r3], s88 offen lds\n\t"
    "s_add_u32 s88, s88, %[st34]\n\t"
    "s_cmp_eq_u32 s88, s48\n\t"
    "s_cselect_b32 s88, s89, s88\n\t"
    "v_mfma_f32_32x32x16_bf16 v[16:31], v[140:143], v[92:95], v[16:31]\n\t"
    "ds_read_b64_tr_b16 v[156:157], v221 offset:6144\n\t"
    "ds_read_b64_tr_b16 v[158:159], v221 offset:6656\n\t"
    "s_waitcnt vmcnt(9)\n\t"
    "s_waitcnt lgkmcnt(0)\n\t"
    "s_barrier\n\t"
    "v_mfma_f32_32x32x16_bf16 v[0:15], v[144:147], v[96:99], v[0:15]\n\t"
    "s_add_u32 m0, %[ldw], 139264\n\t"
    "v_add_u32 v232, 32768, v232\n\t"
    "ds_read_b128 v[104:107], v232\n\t"
    "v_add_u32 v220, 32768, v220\n\t"
    "ds_read_b64_tr_b16 v[136:137], v220\n\t"
    "ds_read_b64_tr_b16 v[138:139], v220 offset:512\n\t"
    "buffer_load_dwordx4 v218, %[r3], s88 offen lds\n\t"
    "s_add_u32 s88, s88, %[st34]\n\t"
    "s_cmp_eq_u32 s88, s48\n\t"
    "s_cselect_b32 s88, s89, s88\n\t"
    "v_mfma_f32_32x32x16_bf16 v[16:31], v[148:151], v[96:99], v[16:31]\n\t"
    "v_add_u32 v221, 32768, v221\n\t"
    "ds_read_b64_tr_b16 v[140:141], v221\n\t"
    "ds_read_b64_tr_b16 v[142:143], v221 offset:512\n\t"
    "v_mfma_f32_32x32x16_bf16 v[0:15], v[152:155], v[100:103], v[0:15]\n\t"
    "s_add_u32 m0, %[ldw], 147456\n\t"
    "v_add_u32 v233, 32768, v233\n\t"
    "ds_read_b128 v[108:111], v233\n\t"
    "ds_read_b64_tr_b16 v[144:145], v220 offset:2048\n\t"
    "ds_read_b64_tr_b16 v[146:147], v220 offset:2560\n\t"
    "buffer_load_dwordx4 v218, %[r3], s88 offen lds\n\t"
    "s_add_u32 s88, s88, %[st34]\n\t"
    "s_cmp_eq_u32 s88, s48\n\t"
    "s_cselect_b32 s88, s89, s88\n\t"
    "v_mfma_f32_32x32x16_bf16 v[16:31], v[156:159], v[100:103], v[16:31]\n\t"
    "ds_read_b64_tr_b16 v[148:149], v221 offset:2048\n\t"
    "ds_read_b64_tr_b16 v[150:151], v221 offset:2560\n\t"
    "s_waitcnt lgkmcnt(5)\n\t"
    "v_mfma_f32_32x32x16_bf16 v[0:15], v[136:139], v[104:107], v[0:15]\n\t"
    "s_add_u32 m0, %[ldw], 155648\n\t"
    "v_add_u32 v234, 32768, v234\n\t"
    "ds_read_b128 v[112:115], v234\n\t"
    "ds_read_b64_tr_b16 v[152:153], v220 offset:4096\n\t"
    "ds_read_b64_tr_b16 v[154:155], v220 offset:4608\n\t"
    "buffer_load_dwordx4 v218, %[r3], s88 offen lds\n\t"
    "s_add_u32 s88, s88, %[st34]\n\t"
    "s_cmp_eq_u32 s88, s48\n\t"
    "s_cselect_b32 s88, s89, s88\n\t"
    "v_mfma_f32_32x32x16_bf16 v[16:31], v[140:143], v[104:107], v[16:31]\n\t"
    "ds_read_b64_tr_b16 v[156:157], v221 offset:4096\n\t"
    "ds_read_b64_tr_b16 v[158:159], v221 offset:4608\n\t"
    "s_waitcnt lgkmcnt(5)\n\t"
    "v_mfma_f32_32x32x16_bf16 v[0:15], v[144:147], v[108:111], v[0:15]\n\t"
    "s_add_u32 m0, %[ldw], 0\n\t"
    "v_add_u32 v235, 32768, v235\n\t"
    "ds_read_b128 v[116:119], v235\n\t"
    "ds_read_b64_tr_b16 v[136:137], v220 offset:6144\n\t"
    "ds_read_b64_tr_b16 v[138:139], v220 offset:6656\n\t"
    "buffer_load_dwordx4 v218, %[r3], s88 offen lds\n\t"
    "s_add_u32 s88, s88, %[st34]\n\t"
    "s_cmp_eq_u32 s88, s48\n\t"
    "s_cselect_b32 s88, s89, s88\n\t"
    "v_mfma_f32_32x32x16_bf16 v[16:31], v[148:151], v[108:111], v[16:31]\n\t"
    "ds_read_b64_tr_b16 v[140:141], v221 offset:6144\n\t"
    "ds_read_b64_tr_b16 v[142:143], v221 offset:6656\n\t"
    "s_waitcnt vmcnt(6)\n\t"
    "s_waitcnt lgkmcnt(0)\n\t"
    "s_barrier\n\t"
    "v_mfma_f32_32x32x16_bf16 v[0:15], v[152:155], v[112:115], v[0:15]\n\t"
    "s_add_u32 m0, %[ldw], 8192\n\t"
    "v_add_u32 v221, 8192, v221\n\t"
    "ds_read_b64_tr_b16 v[144:145], v221\n\t"
    "ds_read_b64_tr_b16 v[146:147], v221 offset:512\n\t"
    "buffer_load_dwordx4 v218, %[r3], s88 offen lds\n\t"
    "s_add_u32 s88, s88, %[st34]\n\t"
    "s_cmp_eq_u32 s88, s48\n\t"
    "s_cselect_b32 s88, s89, s88\n\t"
    "v_mfma_f32_32x32x16_bf16 v[16:31], v[156:159], v[112:115], v[16:31]\n\t"
    "ds_read_b64_tr_b16 v[148:149], v221 offset:2048\n\t"
    "ds_read_b64_tr_b16 v[150:151], v221 offset:2560\n\t"
    "v_mfma_f32_32x32x16_bf16 v[0:15], v[136:139], v[116:119], v[0:15]\n\t"
    "ds_read_b64_tr_b16 v[152:153], v221 offset:4096\n\t"
    "ds_read_b64_tr_b16 v[154:155], v221 offset:4608\n\t"
    "v_mfma_f32_32x32x16_bf16 v[16:31], v[140:143], v[116:119], v[16:31]\n\t"
    "ds_read_b64_tr_b16 v[156:157], v221 offset:6144\n\t"
    "ds_read_b64_tr_b16 v[158:159], v221 offset:6656\n\t"
    "s_waitcnt lgkmcnt(4)\n\t"
    "v_mfma_f32_32x32x16_bf16 v[32:47], v[144:147], v[168:171], 0\n\t"
    "s_add_u32 m0, %[ldw], 16384\n\t"
    "v_add_u32 v221, 8192, v221\n\t"
    "ds_read_b64_tr_b16 v[136:137], v221\n\t"
    "ds_read_b64_tr_b16 v[138:139], v221 offset:512\n\t"
    "buffer_load_dwordx4 v219, %[r4], s88 offen lds\n\t"
    "s_add_u32 s88, s88, %[st34]\n\t"
    "s_cmp_eq_u32 s88, s48\n\t"
    "s_cselect_b32 s88, s89, s88\n\t"
    "v_mfma_f32_32x32x16_bf16 v[32:47], v[148:151], v[172:175], v[32:47]\n\t"
    "ds_read_b64_tr_b16 v[140:141], v221 offset:2048\n\t"
    "ds_read_b64_tr_b16 v[142:143], v221 offset:2560\n\t"
    "s_waitcnt lgkmcnt(4)\n\t"
    "v_mfma_f32_32x32x16_bf16 v[32:47], v[152:155], v[224:227], v[32:47]\n\t"
    "s_add_u32 m0, %[ldw], 24576\n\t"
    "ds_read_b64_tr_b16 v[144:145], v221 offset:4096\n\t"
    "ds_read_b64_tr_b16 v[146:147], v221 offset:4608\n\t"
    "buffer_load_dwordx4 v219, %[r4], s88 offen lds\n\t"
    "s_add_u32 s88, s88, %[st34]\n\t"
    "s_cmp_eq_u32 s88, s48\n\t"
    "s_cselect_b32 s88, s89, s88\n\t"
    "v_mfma_f32_32x32x16_bf16 v[32:47], v[156:159], v[228:231], v[32:47]\n\t"
    "ds_read_b64_tr_b16 v[148:149], v221 offset:6144\n\t"
    "ds_read_b64_tr_b16 v[150:151], v221 offset:6656\n\t"
    "v_mul_f32 v0, s73, v0\n\t"
    "v_mul_f32 v1, s73, v1\n\t"
    "v_mul_f32 v2, s73, v2\n\t"
    "v_mul_f32 v3, s73, v3\n\t"
    "v_mul_f32 v4, s73, v4\n\t"
    "s_waitcnt lgkmcnt(4)\n\t"
    "v_mfma_f32_32x32x16_bf16 v[32:47], v[136:139], v[184:187], v[32:47]\n\t"
    "v_add_u32 v221, 8192, v221\n\t"
    "ds_read_b64_tr_b16 v[152:153], v221\n\t"
    "ds_read_b64_tr_b16 v[154:155], v221 offset:512\n\t"
    "v_mul_f32 v5, s73, v5\n\t"
    "v_mul_f32 v6, s73, v6\n\t"
    "v_mul_f32 v7, s73, v7\n\t"
    "v_mul_f32 v8, s73, v8\n\t"
    "v_mfma_f32_32x32x16_bf16 v[32:47], v[140:143], v[188:191], v[32:47]\n\t"
    "ds_read_b64_tr_b16 v[156:157], v221 offset:2048\n\t"
    "ds_read_b64_tr_b16 v[158:159], v221 offset:2560\n\t"
    "v_mul_f32 v9, s73, v9\n\t"
    "v_mul_f32 v10, s73, v10\n\t"
    "v_mul_f32 v11, s73, v11\n\t"
    "v_mul_f32 v12, s73, v12\n\t"
    "v_mul_f32 v13, s73, v13\n\t"
    "v_mul_f32 v14, s73, v14\n\t"
    "s_waitcnt lgkmcnt(4)\n\t"
    "v_mfma_f32_32x32x16_bf16 v[32:47], v[144:147], v[176:179], v[32:47]\n\t"
    "ds_read_b64_tr_b16 v[136:137], v221 offset:4096\n\t"
    "ds_read_b64_tr_b16 v[138:139], v221 offset:4608\n\t"
    "v_mul_f32 v15, s73, v15\n\t"
    "v_cvt_pk_bf16_f32 v160, v0, v1\n\t"
    "v_cvt_pk_bf16_f32 v161, v2, v3\n\t"
    "v_cvt_pk_bf16_f32 v166, v4, v5\n\t"
    "v_cvt_pk_bf16_f32 v167, v6, v7\n\t"
    "v_mfma_f32_32x32x16_bf16 v[32:47], v[148:151], v[180:183], v[32:47]\n\t"
    "ds_read_b64_tr_b16 v[140:141], v221 offset:6144\n\t"
    "ds_read_b64_tr_b16 v[142:143], v221 offset:6656\n\t"
    "v_cndmask_b32 v162, v166, v164, s[78:79]\n\t"
    "v_cndmask_b32 v163, v167, v165, s[78:79]\n\t"
    "buffer_store_dwordx4 v[160:163], v236, %[rw], 0 offen sc1\n\t"
    "s_waitcnt lgkmcnt(4)\n\t"
    "v_mfma_f32_32x32x16_bf16 v[32:47], v[152:155], v[192:195], v[32:47]\n\t"
    "v_add_u32 v221, 8192, v221\n\t"
    "ds_read_b64_tr_b16 v[144:145], v221\n\t"
    "ds_read_b64_tr_b16 v[146:147], v221 offset:512\n\t"
    "buffer_store_dwordx4 v[164:167], v238, %[rw], 0 offen sc1\n\t"
    "v_mfma_f32_32x32x16_bf16 v[32:47], v[156:159], v[196:199], v[32:47]\n\t"
    "ds_read_b64_tr_b16 v[148:149], v221 offset:2048\n\t"
    "ds_read_b64_tr_b16 v[150:151], v221 offset:2560\n\t"
    "v_cvt_pk_bf16_f32 v160, v8, v9\n\t"
    "v_cvt_pk_bf16_f32 v161, v10, v11\n\t"
    "v_cvt_pk_bf16_f32 v166, v12, v13\n\t"
    "v_cvt_pk_bf16_f32 v167, v14, v15\n\t"
    "v_cndmask_b32 v162, v166, v164, s[54:55]\n\t"
    "v_cndmask_b32 v163, v167, v165, s[54:55]\n\t"
    "s_waitcnt lgkmcnt(4)\n\t"
    "v_mfma_f32_32x32x16_bf16 v[32:47], v[136:139], v[200:203], v[32:47]\n\t"
    "ds_read_b64_tr_b16 v[152:153], v221 offset:4096\n\t"
    "ds_read_b64_tr_b16 v[154:155], v221 offset:4608\n\t"
    "buffer_store_dwordx4 v[160:163], v237, %[rw], 0 offen sc1\n\t"
    "buffer_store_dwordx4 v[164:167], v239, %[rw], 0 offen sc1\n\t"
    "v_mfma_f32_32x32x16_bf16 v[32:47], v[140:143], v[204:207], v[32:47]\n\t"
    "ds_read_b64_tr_b16 v[156:157], v221 offset:6144\n\t"
    "ds_read_b64_tr_b16 v[158:159], v221 offset:6656\n\t"
    "v_mul_f32 v160, 0xbfb8aa3b, v16\n\t"
    "v_exp_f32 v160, v160\n\t"
    "v_mul_f32 v161, 0xbfb8aa3b, v17\n\t"
    "s_waitcnt lgkmcnt(4)\n\t"
    "v_mfma_f32_32x32x16_bf16 v[32:47], v[144:147], v[208:211], v[32:47]\n\t"
    "v_add_u32 v221, 8192, v221\n\t"
    "ds_read_b64_tr_b16 v[136:137], v221\n\t"
    "ds_read_b64_tr_b16 v[138:139], v221 offset:512\n\t"
    "v_exp_f32 v161, v161\n\t"
    "v_add_f32 v160, 1.0, v160\n\t"
    "v_rcp_f32 v160, v160\n\t"
    "v_mfma_f32_32x32x16_bf16 v[32:47], v[148:151], v[212:215], v[32:47]\n\t"
    "ds_read_b64_tr_b16 v[140:141], v221 offset:2048\n\t"
    "ds_read_b64_tr_b16 v[142:143], v221 offset:2560\n\t"
    "v_mul_f32 v162, 0xbfb8aa3b, v18\n\t"
    "v_exp_f32 v162, v162\n\t"
    "v_add_f32 v161, 1.0, v161\n\t"
    "v_rcp_f32 v161, v161\n\t"
    "v_mul_f32 v16, v16, v160\n\t"
    "s_waitcnt lgkmcnt(4)\n\t"
    "v_mfma_f32_32x32x16_bf16 v[32:47], v[152:155], v[64:67], v[32:47]\n\t"
    "s_add_u32 m0, %[ldw], 32768\n\t"
    "ds_read_b64_tr_b16 v[144:145], v221 offset:4096\n\t"
    "ds_read_b64_tr_b16 v[146:147], v221 offset:4608\n\t"
    "buffer_load_dwordx4 v219, %[r4], s88 offen lds\n\t"
    "s_add_u32 s88, s88, %[st34]\n\t"
    "s_cmp_eq_u32 s88, s48\n\t"
    "s_cselect_b32 s88, s89, s88\n\t"
    "v_mfma_f32_32x32x16_bf16 v[32:47], v[156:159], v[68:71], v[32:47]\n\t"
    "ds_read_b64_tr_b16 v[148:149], v221 offset:6144\n\t"
    "ds_read_b64_tr_b16 v[150:151], v221 offset:6656\n\t"
    "v_mul_f32 v163, 0xbfb8aa3b, v19\n\t"
    "v_exp_f32 v163, v163\n\t"
    "v_add_f32 v162, 1.0, v162\n\t"
    "v_rcp_f32 v162, v162\n\t"
    "s_waitcnt lgkmcnt(4)\n\t"
    "v_mfma_f32_32x32x16_bf16 v[32:47], v[136:139], v[72:75], v[32:47]\n\t"
    "s_add_u32 m0, %[ldw], 40960\n\t"
    "v_add_u32 v221, 8192, v221\n\t"
    "ds_read_b64_tr_b16 v[152:153], v221\n\t"
    "ds_read_b64_tr_b16 v[154:155], v221 offset:512\n\t"
    "buffer_load_dwordx4 v219, %[r4], s88 offen lds\n\t"
    "s_add_u32 s88, s88, %[st34]\n\t"
    "s_cmp_eq_u32 s88, s48\n\t"
    "s_cselect_b32 s88, s89, s88\n\t"
    "v_mfma_f32_32x32x16_bf16 v[32:47], v[140:143], v[76:79], v[32:47]\n\t"
    "ds_read_b64_tr_b16 v[156:157], v221 offset:2048\n\t"
    "ds_read_b64_tr_b16 v[158:159], v221 offset:2560\n\t"
    "v_mul_f32 v17, v17, v161\n\t"
    "v_mul_f32 v160, 0xbfb8aa3b, v20\n\t"
    "v_exp_f32 v160, v160\n\t"
    "v_add_f32 v163, 1.0, v163\n\t"
    "s_waitcnt lgkmcnt(4)\n\t"
    "v_mfma_f32_32x32x16_bf16 v[32:47], v[144:147], v[80:83], v[32:47]\n\t"
    "s_add_u32 m0, %[ldw], 49152\n\t"
    "ds_read_b64_tr_b16 v[136:137], v221 offset:4096\n\t"
    "ds_read_b64_tr_b16 v[138:139], v221 offset:4608\n\t"
    "buffer_load_dwordx4 v219, %[r4], s88 offen lds\n\t"
    "s_add_u32 s88, s88, %[st34]\n\t"
    "s_cmp_eq_u32 s88, s48\n\t"
    "s_cselect_b32 s88, s89, s88\n\t"
    "v_mfma_f32_32x32x16_bf16 v[32:47], v[148:151], v[84:87], v[32:47]\n\t"
    "ds_read_b64_tr_b16 v[140:141], v221 offset:6144\n\t"
    "ds_read_b64_tr_b16 v[142:143], v221 offset:6656\n\t"
    "v_rcp_f32 v163, v163\n\t"
    "v_mul_f32 v18, v18, v162\n\t"
    "v_mul_f32 v161, 0xbfb8aa3b, v21\n\t"
    "v_exp_f32 v161, v161\n\t"
    "s_waitcnt lgkmcnt(4)\n\t"
    "v_mfma_f32_32x32x16_bf16 v[32:47], v[152:155], v[88:91], v[32:47]\n\t"
    "s_add_u32 m0, %[ldw], 57344\n\t"
    "v_add_u32 v221, 8192, v221\n\t"
    "ds_read_b64_tr_b16 v[144:145], v221\n\t"
    "ds_read_b64_tr_b16 v[146:147], v221 offset:512\n\t"
    "buffer_load_dwordx4 v219, %[r4], s88 offen lds\n\t"
    "s_add_u32 s88, s88, %[st34]\n\t"
    "s_cmp_eq_u32 s88, s48\n\t"
    "s_cselect_b32 s88, s89, s88\n\t"
    "v_mfma_f32_32x32x16_bf16 v[32:47], v[156:159], v[92:95], v[32:47]\n\t"
    "ds_read_b64_tr_b16 v[148:149], v221 offset:2048\n\t"
    "ds_read_b64_tr_b16 v[150:151], v221 offset:2560\n\t"
    "v_add_f32 v160, 1.0, v160\n\t"
    "v_rcp_f32 v160, v160\n\t"
    "v_mul_f32 v19, v19, v163\n\t"
    "v_mul_f32 v162, 0xbfb8aa3b, v22\n\t"
    "s_waitcnt lgkmcnt(4)\n\t"
    "v_mfma_f32_32x32x16_bf16 v[32:47], v[136:139], v[96:99], v[32:47]\n\t"
    "ds_read_b64_tr_b16 v[152:153], v221 offset:4096\n\t"
    "ds_read_b64_tr_b16 v[154:155], v221 offset:4608\n\t"
    "v_exp_f32 v162, v162\n\t"
    "v_add_f32 v161, 1.0, v161\n\t"
    "v_rcp_f32 v161, v161\n\t"
    "v_mul_f32 v20, v20, v160\n\t"
    "v_mfma_f32_32x32x16_bf16 v[32:47], v[140:143], v[100:103], v[32:47]\n\t"
    "ds_read_b64_tr_b16 v[156:157], v221 offset:6144\n\t"
    "ds_read_b64_tr_b16 v[158:159], v221 offset:6656\n\t"
    "v_mul_f32 v163, 0xbfb8aa3b, v23\n\t"
    "v_exp_f32 v163, v163\n\t"
    "v_add_f32 v162, 1.0, v162\n\t"
    "v_rcp_f32 v162, v162\n\t"
    "v_mul_f32 v21, v21, v161\n\t"
    "s_waitcnt vmcnt(10)\n\t"
    "s_barrier\n\t"
    "v_readfirstlane_b32 s84, v120\n\t"
    "v_readfirstlane_b32 s85, v121\n\t"
    "s_nop 0\n\t"
    "s_add_u32 s84, s84, 1\n\t"
    "s_addc_u32 s85, s85, 0\n\t"
    "s_waitcnt lgkmcnt(4)\n\t"
    "v_mfma_f32_32x32x16_bf16 v[32:47], v[144:147], v[104:107], v[32:47]\n\t"
    "v_add_u32 v221, 8192, v221\n\t"
    "ds_read_b64_tr_b16 v[136:137], v221\n\t"
    "ds_read_b64_tr_b16 v[138:139], v221 offset:512\n\t"
    "v_mul_f32 v160, 0xbfb8aa3b, v24\n\t"
    "v_exp_f32 v160, v160\n\t"
    "v_add_f32 v163, 1.0, v163\n\t"
    "v_mfma_f32_32x32x16_bf16 v[32:47], v[148:151], v[108:111], v[32:47]\n\t"
    "s_add_u32 m0, %[ldw], 65536\n\t"
    "ds_read_b64_tr_b16 v[140:141], v221 offset:2048\n\t"
    "ds_read_b64_tr_b16 v[142:143], v221 offset:2560\n\t"
    "buffer_load_dwordx4 v219, %[r4], s88 offen lds\n\t"
    "s_add_u32 s88, s88, %[st34]\n\t"
    "s_cmp_eq_u32 s88, s48\n\t"
    "s_cselect_b32 s88, s89, s88\n\t"
    "s_waitcnt lgkmcnt(4)\n\t"
    "v_mfma_f32_32x32x16_bf16 v[32:47], v[152:155], v[112:115], v[32:47]\n\t"
    "ds_read_b64_tr_b16 v[144:145], v221 offset:4096\n\t"
    "ds_read_b64_tr_b16 v[146:147], v221 offset:4608\n\t"
    "v_rcp_f32 v163, v163\n\t"
    "v_mul_f32 v22, v22, v162\n\t"
    "v_mul_f32 v161, 0xbfb8aa3b, v25\n\t"
    "v_exp_f32 v161, v161\n\t"
    "v_mfma_f32_32x32x16_bf16 v[32:47], v[156:159], v[116:119], v[32:47]\n\t"
    "ds_read_b64_tr_b16 v[148:149], v221 offset:6144\n\t"
    "ds_read_b64_tr_b16 v[150:151], v221 offset:6656\n\t"
    "v_lshlrev_b32 v241, 3, v252\n\t"
    "s_mov_b64 exec, 0xff\n\t"
    "s_lshl_b32 s80, %[bid], 13\n\t"
    "s_add_u32 s80, s80, %[ldw]\n\t"
    "s_cmp_lt_u32 s80, 1605632\n\t"
    "s_cbranch_scc1 Ltda_%=\n\t"
    "s_add_u32 s80, s80, -1605632\n\t"
    "buffer_load_dword v241, v241, %[ro], s80 offen\n\t"
    "s_branch Ltea_%=\n\t"
    "Ltda_%=:\n\t"
    "buffer_load_dword v241, v241, %[rd], s80 offen\n\t"
    "Ltea_%=:\n\t"
    "s_mov_b64 exec, -1\n\t"
    "s_waitcnt vmcnt(6)\n\t"
    "s_barrier\n\t"
    "s_cmp_eq_u32 %[wv], 0\n\t"
    "s_cbranch_scc0 Lpubpa_%=\n\t"
    "s_mov_b64 exec, 1\n\t"
    "s_add_u32 s80, s71, 0\n\t"
    "v_mov_b32 v164, s84\n\t"
    "v_mov_b32 v165, s85\n\t"
    "buffer_store_dwordx2 v[164:165], off, %[rw], s80 sc1\n\t"
    "s_mov_b64 exec, -1\n\t"
    "Lpubpa_%=:\n\t"
    "s_waitcnt lgkmcnt(4)\n\t"
    "v_mfma_f32_32x32x16_bf16 v[48:63], v[136:139], v[168:171], 0\n\t"
    "v_add_u32 v221, 8192, v221\n\t"
    "ds_read_b64_tr_b16 v[152:153], v221\n\t"
    "ds_read_b64_tr_b16 v[154:155], v221 offset:512\n\t"
    "v_add_f32 v160, 1.0, v160\n\t"
    "v_rcp_f32 v160, v160\n\t"
    "v_mul_f32 v23, v23, v163\n\t"
    "v_mfma_f32_32x32x16_bf16 v[48:63], v[140:143], v[172:175], v[48:63]\n\t"
    "ds_read_b64_tr_b16 v[156:157], v221 offset:2048\n\t"
    "ds_read_b64_tr_b16 v[158:159], v221 offset:2560\n\t"
    "v_mul_f32 v162, 0xbfb8aa3b, v26\n\t"
    "v_exp_f32 v162, v162\n\t"
    "v_add_f32 v161, 1.0, v161\n\t"
    "v_rcp_f32 v161, v161\n\t"
    "v_mul_f32 v24, v24, v160\n\t"
    "s_waitcnt lgkmcnt(4)\n\t"
    "v_mfma_f32_32x32x16_bf16 v[48:63], v[144:147], v[224:227], v[48:63]\n\t"
    "ds_read_b64_tr_b16 v[136:137], v221 offset:4096\n\t"
    "ds_read_b64_tr_b16 v[138:139], v221 offset:4608\n\t"
    "v_mul_f32 v163, 0xbfb8aa3b, v27\n\t"
    "v_exp_f32 v163, v163\n\t"
    "v_add_f32 v162, 1.0, v162\n\t"
    "v_rcp_f32 v162, v162\n\t"
    "v_mfma_f32_32x32x16_bf16 v[48:63], v[148:151], v[228:231], v[48:63]\n\t"
    "ds_read_b64_tr_b16 v[140:141], v221 offset:6144\n\t"
    "ds_read_b64_tr_b16 v[142:143], v221 offset:6656\n\t"
    "v_mul_f32 v25, v25, v161\n\t"
    "v_mul_f32 v160, 0xbfb8aa3b, v28\n\t"
    "v_exp_f32 v160, v160\n\t"
    "v_add_f32 v163, 1.0, v163\n\t"
    "v_rcp_f32 v163, v163\n\t"
    "s_waitcnt lgkmcnt(4)\n\t"
    "v_mfma_f32_32x32x16_bf16 v[48:63], v[152:155], v[184:187], v[48:63]\n\t"
    "v_add_u32 v221, 8192, v221\n\t"
    "ds_read_b64_tr_b16 v[144:145], v221\n\t"
    "ds_read_b64_tr_b16 v[146:147], v221 offset:512\n\t"
    "v_mul_f32 v26, v26, v162\n\t"
    "v_mul_f32 v161, 0xbfb8aa3b, v29\n\t"
    "v_exp_f32 v161, v161\n\t"
    "v_mfma_f32_32x32x16_bf16 v[48:63], v[156:159], v[188:191], v[48:63]\n\t"
    "ds_read_b64_tr_b16 v[148:149], v221 offset:2048\n\t"
    "ds_read_b64_tr_b16 v[150:151], v221 offset:2560\n\t"
    "v_add_f32 v160, 1.0, v160\n\t"
    "v_rcp_f32 v160, v160\n\t"
    "v_mul_f32 v27, v27, v163\n\t"
    "v_mul_f32 v162, 0xbfb8aa3b, v30\n\t"
    "v_exp_f32 v162, v162\n\t"
    "s_waitcnt lgkmcnt(4)\n\t"
    "v_mfma_f32_32x32x16_bf16 v[48:63], v[136:139], v[176:179], v[48:63]\n\t"
    "ds_read_b64_tr_b16 v[152:153], v221 offset:4096\n\t"
    "ds_read_b64_tr_b16 v[154:155], v221 offset:4608\n\t"
    "v_add_f32 v161, 1.0, v161\n\t"
    "v_rcp_f32 v161, v161\n\t"
    "v_mul_f32 v28, v28, v160\n\t"
    "v_mul_f32 v163, 0xbfb8aa3b, v31\n\t"
    "v_mfma_f32_32x32x16_bf16 v[48:63], v[140:143], v[180:183], v[48:63]\n\t"
    "ds_read_b64_tr_b16 v[156:157], v221 offset:6144\n\t"
    "ds_read_b64_tr_b16 v[158:159], v221 offset:6656\n\t"
    "v_exp_f32 v163, v163\n\t"
    "v_add_f32 v162, 1.0, v162\n\t"
    "v_rcp_f32 v162, v162\n\t"
    "v_mul_f32 v29, v29, v161\n\t"
    "v_add_f32 v163, 1.0, v163\n\t"
    "s_waitcnt lgkmcnt(4)\n\t"
    "v_mfma_f32_32x32x16_bf16 v[48:63], v[144:147], v[192:195], v[48:63]\n\t"
    "v_add_u32 v221, 8192, v221\n\t"
    "ds_read_b64_tr_b16 v[136:137], v221\n\t"
    "ds_read_b64_tr_b16 v[138:139], v221 offset:512\n\t"
    "v_rcp_f32 v163, v163\n\t"
    "v_mul_f32 v30, v30, v162\n\t"
    "v_mul_f32 v31, v31, v163\n\t"
    "v_mfma_f32_32x32x16_bf16 v[48:63], v[148:151], v[196:199], v[48:63]\n\t"
    "ds_read_b64_tr_b16 v[140:141], v221 offset:2048\n\t"
    "ds_read_b64_tr_b16 v[142:143], v221 offset:2560\n\t"
    "v_mul_f32 v16, v16, v32\n\t"
    "v_mul_f32 v17, v17, v33\n\t"
    "v_mul_f32 v18, v18, v34\n\t"
    "v_mul_f32 v19, v19, v35\n\t"
    "v_mul_f32 v20, v20, v36\n\t"
    "v_mul_f32 v21, v21, v37\n\t"
    "s_waitcnt lgkmcnt(4)\n\t"
    "v_mfma_f32_32x32x16_bf16 v[48:63], v[152:155], v[200:203], v[48:63]\n\t"
    "ds_read_b64_tr_b16 v[144:145], v221 offset:4096\n\t"
    "ds_read_b64_tr_b16 v[146:147], v221 offset:4608\n\t"
    "v_mul_f32 v22, v22, v38\n\t"
    "v_mul_f32 v23, v23, v39\n\t"
    "v_cvt_pk_bf16_f32 v232, v16, v17\n\t"
    "v_cvt_pk_bf16_f32 v233, v18, v19\n\t"
    "v_cvt_pk_bf16_f32 v234, v20, v21\n\t"
    "v_mfma_f32_32x32x16_bf16 v[48:63], v[156:159], v[204:207], v[48:63]\n\t"
    "ds_read_b64_tr_b16 v[148:149], v221 offset:6144\n\t"
    "ds_read_b64_tr_b16 v[150:151], v221 offset:6656\n\t"
    "v_cvt_pk_bf16_f32 v235, v22, v23\n\t"
    "v_mul_f32 v24, v24, v40\n\t"
    "v_mul_f32 v25, v25, v41\n\t"
    "v_mul_f32 v26, v26, v42\n\t"
    "v_mul_f32 v27, v27, v43\n\t"
    "v_mul_f32 v28, v28, v44\n\t"
    "s_waitcnt lgkmcnt(4)\n\t"
    "v_mfma_f32_32x32x16_bf16 v[48:63], v[136:139], v[208:211], v[48:63]\n\t"
    "v_add_u32 v221, 8192, v221\n\t"
    "ds_read_b64_tr_b16 v[152:153], v221\n\t"
    "ds_read_b64_tr_b16 v[154:155], v221 offset:512\n\t"
    "v_mul_f32 v29, v29, v45\n\t"
    "v_mul_f32 v30, v30, v46\n\t"
    "v_mul_f32 v31, v31, v47\n\t"
    "v_cvt_pk_bf16_f32 v236, v24, v25\n\t"
    "v_mfma_f32_32x32x16_bf16 v[48:63], v[140:143], v[212:215], v[48:63]\n\t"
    "ds_read_b64_tr_b16 v[156:157], v221 offset:2048\n\t"
    "ds_read_b64_tr_b16 v[158:159], v221 offset:2560\n\t"
    "v_cvt_pk_bf16_f32 v237, v26, v27\n\t"
    "v_cvt_pk_bf16_f32 v238, v28, v29\n\t"
    "v_cvt_pk_bf16_f32 v239, v30, v31\n\t"
    "s_waitcnt lgkmcnt(4)\n\t"
    "v_mfma_f32_32x32x16_bf16 v[48:63], v[144:147], v[64:67], v[48:63]\n\t"
    "ds_read_b64_tr_b16 v[136:137], v221 offset:4096\n\t"
    "ds_read_b64_tr_b16 v[138:139], v221 offset:4608\n\t"
    "v_mfma_f32_32x32x16_bf16 v[48:63], v[148:151], v[68:71], v[48:63]\n\t"
    "ds_read_b64_tr_b16 v[140:141], v221 offset:6144\n\t"
    "ds_read_b64_tr_b16 v[142:143], v221 offset:6656\n\t"
    "s_waitcnt lgkmcnt(4)\n\t"
    "v_mfma_f32_32x32x16_bf16 v[48:63], v[152:155], v[72:75], v[48:63]\n\t"
    "v_add_u32 v221, -155648, v221\n\t"
    "ds_read_b64_tr_b16 v[144:145], v221\n\t"
    "ds_read_b64_tr_b16 v[146:147], v221 offset:512\n\t"
    "v_mfma_f32_32x32x16_bf16 v[48:63], v[156:159], v[76:79], v[48:63]\n\t"
    "ds_read_b64_tr_b16 v[148:149], v221 offset:2048\n\t"
    "ds_read_b64_tr_b16 v[150:151], v221 offset:2560\n\t"
    "buffer_load_dwordx2 v[0:1], v245, %[rw], 0 offen sc1\n\t"
    "s_waitcnt lgkmcnt(4)\n\t"
    "v_mfma_f32_32x32x16_bf16 v[48:63], v[136:139], v[80:83], v[48:63]\n\t"
    "ds_read_b64_tr_b16 v[152:153], v221 offset:4096\n\t"
    "ds_read_b64_tr_b16 v[154:155], v221 offset:4608\n\t"
    "buffer_store_dwordx4 v[232:235], v252, %[rw], s65 offen sc1\n\t"
    "v_mfma_f32_32x32x16_bf16 v[48:63], v[140:143], v[84:87], v[48:63]\n\t"
    "ds_read_b64_tr_b16 v[156:157], v221 offset:6144\n\t"
    "ds_read_b64_tr_b16 v[158:159], v221 offset:6656\n\t"
    "s_waitcnt lgkmcnt(4)\n\t"
    "v_mfma_f32_32x32x16_bf16 v[48:63], v[144:147], v[88:91], v[48:63]\n\t"
    "v_add_u32 v221, 8192, v221\n\t"
    "ds_read_b64_tr_b16 v[136:137], v221\n\t"
    "ds_read_b64_tr_b16 v[138:139], v221 offset:512\n\t"
    "v_mfma_f32_32x32x16_bf16 v[48:63], v[148:151], v[92:95], v[48:63]\n\t"
    "ds_read_b64_tr_b16 v[140:141], v221 offset:2048\n\t"
    "ds_read_b64_tr_b16 v[142:143], v221 offset:2560\n\t"
    "buffer_store_dwordx4 v[236:239], v252, %[rw], s58 offen sc1\n\t"
    "s_waitcnt lgkmcnt(4)\n\t"
    "v_mfma_f32_32x32x16_bf16 v[48:63], v[152:155], v[96:99], v[48:63]\n\t"
    "ds_read_b64_tr_b16 v[144:145], v221 offset:4096\n\t"
    "ds_read_b64_tr_b16 v[146:147], v221 offset:4608\n\t"
    "v_mfma_f32_32x32x16_bf16 v[48:63], v[156:159], v[100:103], v[48:63]\n\t"
    "ds_read_b64_tr_b16 v[148:149], v221 offset:6144\n\t"
    "ds_read_b64_tr_b16 v[150:151], v221 offset:6656\n\t"
    "s_waitcnt vmcnt(4)\n\t"
    "s_barrier\n\t"
    "v_mov_b32 v12, 131072\n\t"
    "v_mov_b32 v13, 0\n\t"
    "ds_write_b32 v12, v13\n\t"
    "s_waitcnt lgkmcnt(5)\n\t"
    "v_mfma_f32_32x32x16_bf16 v[48:63], v[136:139], v[104:107], v[48:63]\n\t"
    "v_add_u32 v221, 8192, v221\n\t"
    "ds_read_b64_tr_b16 v[152:153], v221\n\t"
    "ds_read_b64_tr_b16 v[154:155], v221 offset:512\n\t"
    "v_mfma_f32_32x32x16_bf16 v[48:63], v[140:143], v[108:111], v[48:63]\n\t"
    "ds_read_b64_tr_b16 v[156:157], v221 offset:2048\n\t"
    "ds_read_b64_tr_b16 v[158:159], v221 offset:2560\n\t"
    "s_waitcnt lgkmcnt(5)\n\t"
    "v_mfma_f32_32x32x16_bf16 v[48:63], v[144:147], v[112:115], v[48:63]\n\t"
    "ds_read_b64_tr_b16 v[136:137], v221 offset:4096\n\t"
    "ds_read_b64_tr_b16 v[138:139], v221 offset:4608\n\t"
    "v_mfma_f32_32x32x16_bf16 v[48:63], v[148:151], v[116:119], v[48:63]\n\t"
    "ds_read_b64_tr_b16 v[140:141], v221 offset:6144\n\t"
    "ds_read_b64_tr_b16 v[142:143], v221 offset:6656\n\t"
    "s_waitcnt lgkmcnt(4)\n\t"
    "v_mfma_f32_32x32x16_bf16 v[120:135], v[152:155], v[168:171], 0\n\t"
    "v_add_u32 v221, 8192, v221\n\t"
    "ds_read_b64_tr_b16 v[144:145], v221\n\t"
    "ds_read_b64_tr_b16 v[146:147], v221 offset:512\n\t"
    "v_mfma_f32_32x32x16_bf16 v[120:135], v[156:159], v[172:175], v[120:135]\n\t"
    "ds_read_b64_tr_b16 v[148:149], v221 offset:2048\n\t"
    "ds_read_b64_tr_b16 v[150:151], v221 offset:2560\n\t"
    "s_waitcnt vmcnt(2)\n\t"
    "s_mov_b32 s81, 0\n\t"
    "Lpa_%=:\n\t"
    "v_cmp_eq_u64 vcc, s[84:85], v[0:1]\n\t"
    "s_cmp_eq_u32 vcc_lo, -1\n\t"
    "s_cbranch_scc1 Lga_%=\n\t"
    "buffer_load_dwordx2 v[0:1], v245, %[rw], 0 offen sc1\n\t"
    "s_add_u32 s81, s81, 1\n\t"
    "s_waitcnt vmcnt(0)\n\t"
    "s_cmp_lt_u32 s81, 0x60000\n\t"
    "s_cbranch_scc1 Lpa_%=\n\t"
    "Lga_%=:\n\t"
    "s_waitcnt lgkmcnt(4)\n\t"
    "v_mfma_f32_32x32x16_bf16 v[120:135], v[136:139], v[224:227], v[120:135]\n\t"
    "ds_read_b64_tr_b16 v[152:153], v221 offset:4096\n\t"
    "ds_read_b64_tr_b16 v[154:155], v221 offset:4608\n\t"
    "buffer_load_dwordx4 v[168:171], v252, %[rw], s56 offen sc1\n\t"
    "v_mfma_f32_32x32x16_bf16 v[120:135], v[140:143], v[228:231], v[120:135]\n\t"
    "ds_read_b64_tr_b16 v[156:157], v221 offset:6144\n\t"
    "ds_read_b64_tr_b16 v[158:159], v221 offset:6656\n\t"
    "buffer_load_dwordx4 v[172:175], v252, %[rw], s56 offen offset:1024 sc1\n\t"
    "v_mul_f32 v160, 0xbfb8aa3b, v48\n\t"
    "v_exp_f32 v160, v160\n\t"
    "v_mul_f32 v161, 0xbfb8aa3b, v49\n\t"
    "v_exp_f32 v161, v161\n\t"
    "v_add_f32 v160, 1.0, v160\n\t"
    "s_waitcnt lgkmcnt(4)\n\t"
    "v_mfma_f32_32x32x16_bf16 v[120:135], v[144:147], v[184:187], v[120:135]\n\t"
    "v_add_u32 v221, 8192, v221\n\t"
    "ds_read_b64_tr_b16 v[136:137], v221\n\t"
    "ds_read_b64_tr_b16 v[138:139], v221 offset:512\n\t"
    "buffer_load_dwordx4 v[224:227], v252, %[rw], s61 offen sc1\n\t"
    "v_rcp_f32 v160, v160\n\t"
    "v_mfma_f32_32x32x16_bf16 v[120:135], v[148:151], v[188:191], v[120:135]\n\t"
    "ds_read_b64_tr_b16 v[140:141], v221 offset:2048\n\t"
    "ds_read_b64_tr_b16 v[142:143], v221 offset:2560\n\t"
    "buffer_load_dwordx4 v[228:231], v252, %[rw], s61 offen offset:1024 sc1\n\t"
    "v_mul_f32 v162, 0xbfb8aa3b, v50\n\t"
    "v_exp_f32 v162, v162\n\t"
    "v_add_f32 v161, 1.0, v161\n\t"
    "s_waitcnt lgkmcnt(4)\n\t"
    "v_mfma_f32_32x32x16_bf16 v[120:135], v[152:155], v[176:179], v[120:135]\n\t"
    "ds_read_b64_tr_b16 v[144:145], v221 offset:4096\n\t"
    "ds_read_b64_tr_b16 v[146:147], v221 offset:4608\n\t"
    "v_rcp_f32 v161, v161\n\t"
    "v_mul_f32 v48, v48, v160\n\t"
    "v_mul_f32 v163, 0xbfb8aa3b, v51\n\t"
    "v_exp_f32 v163, v163\n\t"
    "v_mfma_f32_32x32x16_bf16 v[120:135], v[156:159], v[180:183], v[120:135]\n\t"
    "ds_read_b64_tr_b16 v[148:149], v221 offset:6144\n\t"
    "ds_read_b64_tr_b16 v[150:151], v221 offset:6656\n\t"
    "buffer_load_dwordx4 v[184:187], v252, %[rw], s50 offen sc1\n\t"
    "v_add_f32 v162, 1.0, v162\n\t"
    "v_rcp_f32 v162, v162\n\t"
    "v_mul_f32 v49, v49, v161\n\t"
    "s_waitcnt lgkmcnt(4)\n\t"
    "v_mfma_f32_32x32x16_bf16 v[120:135], v[136:139], v[192:195], v[120:135]\n\t"
    "v_add_u32 v221, 8192, v221\n\t"
    "ds_read_b64_tr_b16 v[152:153], v221\n\t"
    "ds_read_b64_tr_b16 v[154:155], v221 offset:512\n\t"
    "v_mul_f32 v160, 0xbfb8aa3b, v52\n\t"
    "v_exp_f32 v160, v160\n\t"
    "v_add_f32 v163, 1.0, v163\n\t"
    "v_mfma_f32_32x32x16_bf16 v[120:135], v[140:143], v[196:199], v[120:135]\n\t"
    "ds_read_b64_tr_b16 v[156:157], v221 offset:2048\n\t"
    "ds_read_b64_tr_b16 v[158:159], v221 offset:2560\n\t"
    "buffer_load_dwordx4 v[188:191], v252, %[rw], s50 offen offset:1024 sc1\n\t"
    "v_rcp_f32 v163, v163\n\t"
    "v_mul_f32 v50, v50, v162\n\t"
    "v_mul_f32 v161, 0xbfb8aa3b, v53\n\t"
    "s_waitcnt lgkmcnt(4)\n\t"
    "v_mfma_f32_32x32x16_bf16 v[120:135], v[144:147], v[200:203], v[120:135]\n\t"
    "ds_read_b64_tr_b16 v[136:137], v221 offset:4096\n\t"
    "ds_read_b64_tr_b16 v[138:139], v221 offset:4608\n\t"
    "v_exp_f32 v161, v161\n\t"
    "v_add_f32 v160, 1.0, v160\n\t"
    "v_rcp_f32 v160, v160\n\t"
    "v_mul_f32 v51, v51, v163\n\t"
    "v_mfma_f32_32x32x16_bf16 v[120:135], v[148:151], v[204:207], v[120:135]\n\t"
    "ds_read_b64_tr_b16 v[140:141], v221 offset:6144\n\t"
    "ds_read_b64_tr_b16 v[142:143], v221 offset:6656\n\t"
    "s_add_u32 m0, s70, 16384\n\t"
    "s_add_u32 s80, s59, 0\n\t"
    "buffer_load_dwordx4 v248, %[rd], s80 offen lds\n\t"
    "v_mul_f32 v162, 0xbfb8aa3b, v54\n\t"
    "v_exp_f32 v162, v162\n\t"
    "s_waitcnt lgkmcnt(4)\n\t"
    "v_mfma_f32_32x32x16_bf16 v[120:135], v[152:155], v[208:211], v[120:135]\n\t"
    "v_add_u32 v221, 8192, v221\n\t"
    "ds_read_b64_tr_b16 v[144:145], v221\n\t"
    "ds_read_b64_tr_b16 v[146:147], v221 offset:512\n\t"
    "v_add_f32 v161, 1.0, v161\n\t"
    "v_rcp_f32 v161, v161\n\t"
    "v_mul_f32 v52, v52, v160\n\t"
    "v_mfma_f32_32x32x16_bf16 v[120:135], v[156:159], v[212:215], v[120:135]\n\t"
    "ds_read_b64_tr_b16 v[148:149], v221 offset:2048\n\t"
    "ds_read_b64_tr_b16 v[150:151], v221 offset:2560\n\t"
    "s_add_u32 s80, s59, 6144\n\t"
    "buffer_load_dwordx4 v248, %[rd], s80 offen offset:1024 lds\n\t"
    "v_mul_f32 v163, 0xbfb8aa3b, v55\n\t"
    "v_exp_f32 v163, v163\n\t"
    "v_add_f32 v162, 1.0, v162\n\t"
    "s_waitcnt vmcnt(8)\n\t"
    "s_barrier\n\t"
    "s_cmp_eq_u32 %[wv], 0\n\t"
    "s_cbranch_scc0 Lpubpb0_%=\n\t"
    "s_mov_b64 exec, 1\n\t"
    "s_add_u32 s80, s71, 32768\n\t"
    "v_mov_b32 v164, s84\n\t"
    "v_mov_b32 v165, s85\n\t"
    "buffer_store_dwordx2 v[164:165], off, %[rw], s80 sc1\n\t"
    "s_mov_b64 exec, -1\n\t"
    "Lpubpb0_%=:\n\t"
    "s_waitcnt lgkmcnt(4)\n\t"
    "v_mfma_f32_32x32x16_bf16 v[120:135], v[136:139], v[64:67], v[120:135]\n\t"
    "ds_read_b64_tr_b16 v[152:153], v221 offset:4096\n\t"
    "ds_read_b64_tr_b16 v[154:155], v221 offset:4608\n\t"
    "v_rcp_f32 v162, v162\n\t"
    "v_mul_f32 v53, v53, v161\n\t"
    "v_mul_f32 v160, 0xbfb8aa3b, v56\n\t"
    "v_exp_f32 v160, v160\n\t"
    "v_mfma_f32_32x32x16_bf16 v[120:135], v[140:143], v[68:71], v[120:135]\n\t"
    "ds_read_b64_tr_b16 v[156:157], v221 offset:6144\n\t"
    "ds_read_b64_tr_b16 v[158:159], v221 offset:6656\n\t"
    "s_add_u32 m0, s70, 32768\n\t"
    "s_add_u32 s80, s59, 229376\n\t"
    "buffer_load_dwordx4 v248, %[rd], s80 offen lds\n\t"
    "v_add_f32 v163, 1.0, v163\n\t"
    "v_rcp_f32 v163, v163\n\t"
    "s_waitcnt lgkmcnt(4)\n\t"
    "v_mfma_f32_32x32x16_bf16 v[120:135], v[144:147], v[72:75], v[120:135]\n\t"
    "v_add_u32 v221, 8192, v221\n\t"
    "ds_read_b64_tr_b16 v[136:137], v221\n\t"
    "ds_read_b64_tr_b16 v[138:139], v221 offset:512\n\t"
    "v_mul_f32 v54, v54, v162\n\t"
    "v_mul_f32 v161, 0xbfb8aa3b, v57\n\t"
    "v_exp_f32 v161, v161\n\t"
    "v_mfma_f32_32x32x16_bf16 v[120:135], v[148:151], v[76:79], v[120:135]\n\t"
    "ds_read_b64_tr_b16 v[140:141], v221 offset:2048\n\t"
    "ds_read_b64_tr_b16 v[142:143], v221 offset:2560\n\t"
    "s_add_u32 s80, s59, 235520\n\t"
    "buffer_load_dwordx4 v248, %[rd], s80 offen offset:1024 lds\n\t"
    "v_add_f32 v160, 1.0, v160\n\t"
    "v_rcp_f32 v160, v160\n\t"
    "v_mul_f32 v55, v55, v163\n\t"
    "s_waitcnt lgkmcnt(4)\n\t"
    "v_mfma_f32_32x32x16_bf16 v[120:135], v[152:155], v[80:83], v[120:135]\n\t"
    "ds_read_b64_tr_b16 v[144:145], v221 offset:4096\n\t"
    "ds_read_b64_tr_b16 v[146:147], v221 offset:4608\n\t"
    "v_mul_f32 v162, 0xbfb8aa3b, v58\n\t"
    "v_exp_f32 v162, v162\n\t"
    "v_add_f32 v161, 1.0, v161\n\t"
    "v_rcp_f32 v161, v161\n\t"
    "v_mfma_f32_32x32x16_bf16 v[120:135], v[156:159], v[84:87], v[120:135]\n\t"
    "ds_read_b64_tr_b16 v[148:149], v221 offset:6144\n\t"
    "ds_read_b64_tr_b16 v[150:151], v221 offset:6656\n\t"
    "s_add_u32 m0, s70, 49152\n\t"
    "s_add_u32 s80, s59, 458752\n\t"
    "buffer_load_dwordx4 v248, %[rd], s80 offen lds\n\t"
    "v_mul_f32 v56, v56, v160\n\t"
    "v_mul_f32 v163, 0xbfb8aa3b, v59\n\t"
    "s_waitcnt lgkmcnt(4)\n\t"
    "v_mfma_f32_32x32x16_bf16 v[120:135], v[136:139], v[88:91], v[120:135]\n\t"
    "v_add_u32 v221, 8192, v221\n\t"
    "ds_read_b64_tr_b16 v[152:153], v221\n\t"
    "ds_read_b64_tr_b16 v[154:155], v221 offset:512\n\t"
    "v_exp_f32 v163, v163\n\t"
    "v_add_f32 v162, 1.0, v162\n\t"
    "v_rcp_f32 v162, v162\n\t"
    "v_mfma_f32_32x32x16_bf16 v[120:135], v[140:143], v[92:95], v[120:135]\n\t"
    "ds_read_b64_tr_b16 v[156:157], v221 offset:2048\n\t"
    "ds_read_b64_tr_b16 v[158:159], v221 offset:2560\n\t"
    "s_add_u32 s80, s59, 464896\n\t"
    "buffer_load_dwordx4 v248, %[rd], s80 offen offset:1024 lds\n\t"
    "v_mul_f32 v57, v57, v161\n\t"
    "v_mul_f32 v160, 0xbfb8aa3b, v60\n\t"
    "s_cmp_lt_u32 %[wv], 4\n\t"
    "s_cbranch_scc0 Lnorefa_%=\n\t"
    "s_waitcnt vmcnt(8)\n\t"
    "s_bitcmp1_b32 %[ty], 4\n\t"
    "s_cselect_b32 s94, -1, 0\n\t"
    "s_mov_b32 s95, 0\n\t"
    "s_mov_b32 s96, 0\n\t"
    "s_cselect_b32 s97, 0, -1\n\t"
    "v_cndmask_b32 v224, v224, 0, s[94:95]\n\t"
    "v_cndmask_b32 v225, v225, 0, s[94:95]\n\t"
    "v_cndmask_b32 v230, v230, 0, s[96:97]\n\t"
    "v_cndmask_b32 v231, v231, 0, s[96:97]\n\t"
    "s_lshl_b32 s80, %[wv], 11\n\t"
    "v_add_u32 v242, s80, v246\n\t"
    "v_mfma_f32_32x32x16_bf16 v[16:31], v[168:171], v[224:227], 0\n\t"
    "v_mfma_f32_32x32x16_bf16 v[16:31], v[172:175], v[228:231], v[16:31]\n\t"
    "s_nop 11\n\t"
    "v_max3_f32 v32, v16, v17, v18\n\t"
    "v_max3_f32 v33, v19, v20, v21\n\t"
    "v_max3_f32 v32, v32, v22, v23\n\t"
    "v_max3_f32 v33, v33, v24, v25\n\t"
    "v_max3_f32 v32, v32, v26, v27\n\t"
    "v_max3_f32 v33, v33, v28, v29\n\t"
    "v_max3_f32 v32, v32, v30, v31\n\t"
    "v_max_f32 v32, v32, v33\n\t"
    "v_mov_b32 v33, v32\n\t"
    "s_nop 1\n\t"
    "v_permlane32_swap_b32 v32, v33\n\t"
    "s_nop 1\n\t"
    "v_min_f32 v32, -v32, -v33\n\t"
    "v_and_b32 v33, 0xffff0000, v32\n\t"
    "v_sub_f32 v33, v32, v33\n\t"
    "v_and_b32 v34, 0xffff0000, v33\n\t"
    "v_lshrrev_b32 v32, 16, v32\n\t"
    "v_sub_f32 v33, v33, v34\n\t"
    "v_or_b32 v32, v32, v34\n\t"
    "v_lshrrev_b32 v33, 16, v33\n\t"
    "v_cndmask_b32 v224, v224, v32, s[94:95]\n\t"
    "v_cndmask_b32 v225, v225, v33, s[94:95]\n\t"
    "v_cndmask_b32 v230, v230, v32, s[96:97]\n\t"
    "v_cndmask_b32 v231, v231, v33, s[96:97]\n\t"
    "ds_write_b128 v242, v[224:227]\n\t"
    "ds_write_b128 v242, v[228:231] offset:1024\n\t"
    "Lnorefa_%=:\n\t"
    "s_waitcnt lgkmcnt(4)\n\t"
    "v_mfma_f32_32x32x16_bf16 v[120:135], v[144:147], v[96:99], v[120:135]\n\t"
    "ds_read_b64_tr_b16 v[136:137], v221 offset:4096\n\t"
    "ds_read_b64_tr_b16 v[138:139], v221 offset:4608\n\t"
    "v_exp_f32 v160, v160\n\t"
    "v_add_f32 v163, 1.0, v163\n\t"
    "v_rcp_f32 v163, v163\n\t"
    "v_mul_f32 v58, v58, v162\n\t"
    "v_mfma_f32_32x32x16_bf16 v[120:135], v[148:151], v[100:103], v[120:135]\n\t"
    "ds_read_b64_tr_b16 v[140:141], v221 offset:6144\n\t"
    "ds_read_b64_tr_b16 v[142:143], v221 offset:6656\n\t"
    "v_mul_f32 v161, 0xbfb8aa3b, v61\n\t"
    "v_exp_f32 v161, v161\n\t"
    "v_add_f32 v160, 1.0, v160\n\t"
    "v_rcp_f32 v160, v160\n\t"
    "v_mul_f32 v59, v59, v163\n\t"
    "s_waitcnt lgkmcnt(4)\n\t"
    "v_mfma_f32_32x32x16_bf16 v[120:135], v[152:155], v[104:107], v[120:135]\n\t"
    "v_mul_f32 v162, 0xbfb8aa3b, v62\n\t"
    "v_exp_f32 v162, v162\n\t"
    "v_add_f32 v161, 1.0, v161\n\t"
    "v_rcp_f32 v161, v161\n\t"
    "v_mul_f32 v60, v60, v160\n\t"
    "v_mfma_f32_32x32x16_bf16 v[120:135], v[156:159], v[108:111], v[120:135]\n\t"
    "v_mul_f32 v163, 0xbfb8aa3b, v63\n\t"
    "v_exp_f32 v163, v163\n\t"
    "v_add_f32 v162, 1.0, v162\n\t"
    "v_rcp_f32 v162, v162\n\t"
    "v_mul_f32 v61, v61, v161\n\t"
    "v_add_f32 v163, 1.0, v163\n\t"
    "v_rcp_f32 v163, v163\n\t"
    "s_waitcnt lgkmcnt(0)\n\t"
    "v_mfma_f32_32x32x16_bf16 v[120:135], v[136:139], v[112:115], v[120:135]\n\t"
    "v_mul_f32 v62, v62, v162\n\t"
    "v_mul_f32 v63, v63, v163\n\t"
    "v_mfma_f32_32x32x16_bf16 v[120:135], v[140:143], v[116:119], v[120:135]\n\t"
    "s_branch Ljoin_%=\n\t"
    "Lq_%=:\n\t"
    "s_lshr_b32 s72, %[s2], 1\n\t"
    "s_add_u32 s72, s72, -896\n\t"
    "s_and_b32 s81, %[wv], 4\n\t"
    "s_lshl3_add_u32 s72, s81, s72\n\t"
    "s_lshl_b32 s82, s91, 12\n\t"
    "s_add_u32 s82, s82, 198656\n\t"
    "s_and_b32 s83, %[cs], 1\n\t"
    "s_lshl3_add_u32 s81, s83, s81\n\t"
    "s_lshl_b32 s81, s81, 18\n\t"
    "s_add_u32 s81, s81, s82\n\t"
    "s_add_u32 s81, s81, 448\n\t"
    "v_bfe_u32 v242, v252, 4, 5\n\t"
    "v_and_b32 v243, 0x200, v252\n\t"
    "v_add_u32 v243, s82, v243\n\t"
    "v_and_b32 v32, 0x300, v252\n\t"
    "v_and_b32 v33, 0xc0, v252\n\t"
    "v_and_b32 v34, 0x30, v252\n\t"
    "v_lshl_add_u32 v32, v32, 10, v34\n\t"
    "v_lshl_add_u32 v236, v33, 3, v32\n\t"
    "v_add_u32 v236, s81, v236\n\t"
    "v_mov_b32 v33, 0x3f803f80\n\t"
    "s_mov_b32 s92, 0x11111111\n\t"
    "s_mov_b32 s93, 0x11111111\n\t"
    "v_cndmask_b32 v160, 0, v33, s[92:93]\n\t"
    "v_cndmask_b32 v161, 0, v33, s[92:93]\n\t"
    "v_cndmask_b32 v162, 0, v33, s[92:93]\n\t"
    "v_cndmask_b32 v163, 0, v33, s[92:93]\n\t"
    "s_lshl_b32 s81, s50, 18\n\t"
    "s_lshl_b32 s82, %[wv], 15\n\t"
    "s_add_u32 s81, s81, s82\n\t"
    "s_add_u32 s56, s81, 196608\n\t"
    "s_lshl1_add_u32 s70, %[ldw], 65536\n\t"
    "s_lshl3_add_u32 s81, s52, %[wv]\n\t"
    "s_mul_i32 s81, s81, 28672\n\t"
    "s_add_u32 s57, s81, 8585216\n\t"
    "s_and_b32 s81, %[wv], 4\n\t"
    "s_add_u32 s81, s81, %[wv]\n\t"
    "s_mul_i32 s81, s81, 14336\n\t"
    "s_add_u32 s59, s81, s53\n\t"
    "s_mul_i32 s81, %[wv], 7168\n\t"
    "s_add_u32 s60, s81, s53\n\t"
    "s_mul_i32 s81, s52, 57344\n\t"
    "s_add_u32 s81, s81, s60\n\t"
    "s_mov_b64 s[76:77], %[out]\n\t"
    "s_add_u32 s76, s76, s81\n\t"
    "s_addc_u32 s77, s77, 0\n\t"
    "s_lshl4_add_u32 s81, s50, s51\n\t"
    "s_lshl2_add_u32 s81, s81, %[wv]\n\t"
    "s_lshl_b32 s81, s81, 11\n\t"
    "s_add_u32 s81, s81, 4390912\n\t"
    "s_cmp_lt_u32 %[wv], 4\n\t"
    "s_cselect_b32 s61, s81, 2.0\n\t"
    "s_lshl_b32 s81, s91, 7\n\t"
    "s_add_u32 s62, s81, 16384\n\t"
    "s_and_b32 s82, %[wv], 1\n\t"
    "s_lshl1_add_u32 s83, s52, s82\n\t"
    "s_lshl_b32 s81, s83, 7\n\t"
    "s_add_u32 s63, s81, 16384\n\t"
    "s_lshr_b32 s82, %[wv], 1\n\t"
    "s_lshl2_add_u32 s81, s83, s82\n\t"
    "s_lshl_b32 s81, s81, 13\n\t"
    "s_add_u32 s69, s81, 6488064\n\t"
    "s_lshl3_add_u32 s81, s52, %[wv]\n\t"
    "s_lshl_b32 s81, s81, 5\n\t"
    "s_add_u32 s64, s81, 16384\n\t"
    "s_lshl3_add_u32 s81, s51, %[wv]\n\t"
    "s_lshl_b32 s81, s81, 6\n\t"
    "s_add_u32 s75, s81, 16384\n\t"
    "s_lshr_b32 s81, s91, 1\n\t"
    "s_and_b32 s82, %[cs], 1\n\t"
    "s_lshl1_add_u32 s81, s81, s82\n\t"
    "s_lshr_b32 s82, %[wv], 2\n\t"
    "s_lshl1_add_u32 s81, s81, s82\n\t"
    "s_mul_i32 s81, s81, 28\n\t"
    "s_lshr_b32 s82, %[cs], 1\n\t"
    "s_add_u32 s81, s81, s82\n\t"
    "s_and_b32 s82, s91, 1\n\t"
    "s_lshl1_add_u32 s81, s81, s82\n\t"
    "s_lshl_b32 s81, s81, 10\n\t"
    "s_add_u32 s81, s81, 8585216\n\t"
    "s_bitcmp1_b32 %[ty], 1\n\t"
    "s_cselect_b32 s65, 2.0, s81\n\t"
    "s_xor_b32 s58, s65, 1024\n\t"
    "s_add_u32 s58, s58, 28672\n\t"
    "s_lshr_b32 s82, %[wv], 1\n\t"
    "s_lshl2_add_u32 s81, s51, s82\n\t"
    "s_lshl4_add_u32 s81, s81, s50\n\t"
    "s_and_b32 s83, %[wv], 1\n\t"
    "s_lshl3_add_u32 s81, s81, s83\n\t"
    "s_lshl_b32 s81, s81, 8\n\t"
    "s_add_u32 s66, s81, 6488064\n\t"
    "s_lshl4_add_u32 s81, s51, s50\n\t"
    "s_lshl_b32 s81, s81, 7\n\t"
    "s_add_u32 s67, s81, 131072\n\t"
    "s_lshr_b32 s83, s52, 1\n\t"
    "s_lshl2_add_u32 s81, s83, s82\n\t"
    "s_lshl_b32 s81, s81, 9\n\t"
    "s_add_u32 s68, s81, 131072\n\t"
    "s_cmp_lt_u32 %[bid], 224\n\t"
    "s_cselect_b32 s57, s57, 2.0\n\t"
    "s_cselect_b32 s59, s59, 2.0\n\t"
    "s_cselect_b32 s60, s60, 2.0\n\t"
    "v_xor_b32 v250, 64, v249\n\t"
    "v_add_u32 v246, 139264, v252\n\t"
    "s_lshr_b32 s81, s52, 1\n\t"
    "s_lshl_b32 s81, s81, 11\n\t"
    "s_add_u32 s81, s81, 32768\n\t"
    "s_lshr_b32 s82, %[wv], 2\n\t"
    "s_lshl_b32 s82, s82, 7\n\t"
    "s_add_u32 s82, s82, s81\n\t"
    "s_add_u32 s83, s68, -32768\n\t"
    "v_lshl_add_u32 v251, v252, 4, s82\n\t"
    "v_and_b32 v32, 0x30, v252\n\t"
    "v_lshl_add_u32 v32, v32, 3, s83\n\t"
    "s_mov_b32 s92, 0xfffffe00\n\t"
    "s_mov_b32 s93, 0xffffffff\n\t"
    "v_cndmask_b32 v251, v251, v32, s[92:93]\n\t"
    "s_add_u32 s81, s81, 1920\n\t"
    "s_lshl_b32 s82, s51, 11\n\t"
    "s_add_u32 s82, s82, 34688\n\t"
    "v_writelane_b32 v251, s81, 7\n\t"
    "v_writelane_b32 v251, s82, 8\n\t"
    "s_mul_i32 s81, s50, 28\n\t"
    "s_add_u32 s83, s81, 27\n\t"
    "s_lshr_b32 s81, s81, 6\n\t"
    "s_lshr_b32 s83, s83, 6\n\t"
    "s_sub_u32 s83, s83, s81\n\t"
    "s_lshl_b32 s83, s83, 3\n\t"
    "s_lshl_b32 s81, s81, 7\n\t"
    "s_add_u32 s82, s82, s81\n\t"
    "s_add_u32 s82, s82, -1920\n\t"
    "v_and_b32 v32, 16, v252\n\t"
    "v_and_b32 v34, 64, v252\n\t"
    "v_lshlrev_b32 v34, 5, v34\n\t"
    "v_lshl_add_u32 v33, v32, 3, v34\n\t"
    "v_mul_u32_u24 v32, s83, v32\n\t"
    "s_lshl_b32 s83, %[ldw], 2\n\t"
    "s_add_u32 s81, s81, s83\n\t"
    "s_add_u32 s81, s81, 33664\n\t"
    "s_add_u32 s83, s83, 67328\n\t"
    "v_add_u32 v33, s83, v33\n\t"
    "v_add3_u32 v34, v34, v32, s81\n\t"
    "v_add_u32 v32, s82, v32\n\t"
    "s_mov_b32 s92, 0xcccccccc\n\t"
    "s_mov_b32 s93, 0xcccccccc\n\t"
    "v_cndmask_b32 v32, v34, v32, s[92:93]\n\t"
    "s_mov_b32 s92, 0x0\n\t"
    "s_mov_b32 s93, 0xffffffff\n\t"
    "v_cndmask_b32 v245, v32, v33, s[92:93]\n\t"
    "s_add_u32 s50, s56, 4096\n\t"
    "s_add_u32 s51, s56, 8192\n\t"
    "s_add_u32 s52, s56, 12288\n\t"
    "s_add_u32 s53, s56, 16384\n\t"
    "s_add_u32 m0, %[ldw], 32768\n\t"
    "s_nop 0\n\t"
    "buffer_load_dwordx4 v216, %[rx], s86 offen lds\n\t"
    "s_add_u32 m0, %[ldw], 40960\n\t"
    "s_nop 0\n\t"
    "buffer_load_dwordx4 v241, %[rx], s86 offen lds\n\t"
    "s_add_u32 s86, s86, 128\n\t"
    "s_cmp_eq_u32 s86, s46\n\t"
    "s_cselect_b32 s86, %[sx], s86\n\t"
    "s_add_u32 m0, %[ldw], 49152\n\t"
    "s_nop 0\n\t"
    "buffer_load_dwordx4 v217, %[rq], s87 offen lds\n\t"
    "s_add_u32 s87, s87, 172032\n\t"
    "s_cmp_eq_u32 s87, s47\n\t"
    "s_cselect_b32 s87, %[s2], s87\n\t"
    "s_add_u32 m0, %[ldw], 57344\n\t"
    "s_nop 0\n\t"
    "buffer_load_dwordx4 v218, %[r3], s88 offen lds\n\t"
    "s_add_u32 s88, s88, %[st34]\n\t"
    "s_cmp_eq_u32 s88, s48\n\t"
    "s_cselect_b32 s88, s89, s88\n\t"
    "s_add_u32 m0, %[ldw], 65536\n\t"
    "s_nop 0\n\t"
    "buffer_load_dwordx4 v216, %[rx], s86 offen lds\n\t"
    "s_add_u32 m0, %[ldw], 73728\n\t"
    "s_nop 0\n\t"
    "buffer_load_dwordx4 v241, %[rx], s86 offen lds\n\t"
    "s_add_u32 s86, s86, 128\n\t"
    "s_cmp_eq_u32 s86, s46\n\t"
    "s_cselect_b32 s86, %[sx], s86\n\t"
    "s_waitcnt vmcnt(7)\n\t"
    "s_waitcnt lgkmcnt(0)\n\t"
    "s_barrier\n\t"
    "ds_read_b128 v[168:171], v232\n\t"
    "ds_read_b64_tr_b16 v[136:137], v220\n\t"
    "ds_read_b64_tr_b16 v[138:139], v220 offset:512\n\t"
    "ds_read_b64_tr_b16 v[140:141], v221\n\t"
    "ds_read_b64_tr_b16 v[142:143], v221 offset:512\n\t"
    "ds_read_b128 v[172:175], v233\n\t"
    "ds_read_b64_tr_b16 v[144:145], v220 offset:2048\n\t"
    "ds_read_b64_tr_b16 v[146:147], v220 offset:2560\n\t"
    "ds_read_b64_tr_b16 v[148:149], v221 offset:2048\n\t"
    "ds_read_b64_tr_b16 v[150:151], v221 offset:2560\n\t"
    "s_waitcnt lgkmcnt(5)\n\t"
    "v_mfma_f32_32x32x16_bf16 v[0:15], v[168:171], v[136:139], 0\n\t"
    "s_add_u32 m0, %[ldw], 81920\n\t"
    "ds_read_b128 v[224:227], v234\n\t"
    "ds_read_b64_tr_b16 v[152:153], v220 offset:4096\n\t"
    "ds_read_b64_tr_b16 v[154:155], v220 offset:4608\n\t"
    "buffer_load_dwordx4 v217, %[rq], s87 offen lds\n\t"
    "s_add_u32 s87, s87, 172032\n\t"
    "s_cmp_eq_u32 s87, s47\n\t"
    "s_cselect_b32 s87, %[s2], s87\n\t"
    "v_mfma_f32_32x32x16_bf16 v[16:31], v[168:171], v[140:143], 0\n\t"
    "s_add_u32 m0, %[ldw], 90112\n\t"
    "ds_read_b64_tr_b16 v[156:157], v221 offset:4096\n\t"
    "ds_read_b64_tr_b16 v[158:159], v221 offset:4608\n\t"
    "buffer_load_dwordx4 v218, %[r3], s88 offen lds\n\t"
    "s_add_u32 s88, s88, %[st34]\n\t"
    "s_cmp_eq_u32 s88, s48\n\t"
    "s_cselect_b32 s88, s89, s88\n\t"
    "s_waitcnt lgkmcnt(5)\n\t"
    "v_mfma_f32_32x32x16_bf16 v[0:15], v[172:175], v[144:147], v[0:15]\n\t"
    "s_add_u32 m0, %[ldw], 98304\n\t"
    "ds_read_b128 v[228:231], v235\n\t"
    "ds_read_b64_tr_b16 v[136:137], v220 offset:6144\n\t"
    "ds_read_b64_tr_b16 v[138:139], v220 offset:6656\n\t"
    "buffer_load_dwordx4 v216, %[rx], s86 offen lds\n\t"
    "v_mfma_f32_32x32x16_bf16 v[16:31], v[172:175], v[148:151], v[16:31]\n\t"
    "s_add_u32 m0, %[ldw], 106496\n\t"
    "ds_read_b64_tr_b16 v[140:141], v221 offset:6144\n\t"
    "ds_read_b64_tr_b16 v[142:143], v221 offset:6656\n\t"
    "buffer_load_dwordx4 v241, %[rx], s86 offen lds\n\t"
    "s_add_u32 s86, s86, 128\n\t"
    "s_cmp_eq_u32 s86, s46\n\t"
    "s_cselect_b32 s86, %[sx], s86\n\t"
    "s_waitcnt vmcnt(6)\n\t"
    "s_waitcnt lgkmcnt(0)\n\t"
    "s_barrier\n\t"
    "v_mfma_f32_32x32x16_bf16 v[0:15], v[224:227], v[152:155], v[0:15]\n\t"
    "s_add_u32 m0, %[ldw], 114688\n\t"
    "v_add_u32 v232, 32768, v232\n\t"
    "ds_read_b128 v[184:187], v232\n\t"
    "v_add_u32 v220, 32768, v220\n\t"
    "ds_read_b64_tr_b16 v[144:145], v220\n\t"
    "ds_read_b64_tr_b16 v[146:147], v220 offset:512\n\t"
    "buffer_load_dwordx4 v217, %[rq], s87 offen lds\n\t"
    "s_add_u32 s87, s87, 172032\n\t"
    "s_cmp_eq_u32 s87, s47\n\t"
    "s_cselect_b32 s87, %[s2], s87\n\t"
    "v_mfma_f32_32x32x16_bf16 v[16:31], v[224:227], v[156:159], v[16:31]\n\t"
    "s_add_u32 m0, %[ldw], 122880\n\t"
    "v_add_u32 v221, 32768, v221\n\t"
    "ds_read_b64_tr_b16 v[148:149], v221\n\t"
    "ds_read_b64_tr_b16 v[150:151], v221 offset:512\n\t"
    "buffer_load_dwordx4 v218, %[r3], s88 offen lds\n\t"
    "s_add_u32 s88, s88, %[st34]\n\t"
    "s_cmp_eq_u32 s88, s48\n\t"
    "s_cselect_b32 s88, s89, s88\n\t"
    "v_mfma_f32_32x32x16_bf16 v[0:15], v[228:231], v[136:139], v[0:15]\n\t"
    "s_add_u32 m0, %[ldw], 131072\n\t"
    "v_add_u32 v233, 32768, v233\n\t"
    "ds_read_b128 v[188:191], v233\n\t"
    "ds_read_b64_tr_b16 v[152:153], v220 offset:2048\n\t"
    "ds_read_b64_tr_b16 v[154:155], v220 offset:2560\n\t"
    "buffer_load_dwordx4 v216, %[rx], s86 offen lds\n\t"
    "v_mfma_f32_32x32x16_bf16 v[16:31], v[228:231], v[140:143], v[16:31]\n\t"
    "s_add_u32 m0, %[ldw], 139264\n\t"
    "ds_read_b64_tr_b16 v[156:157], v221 offset:2048\n\t"
    "ds_read_b64_tr_b16 v[158:159], v221 offset:2560\n\t"
    "buffer_load_dwordx4 v241, %[rx], s86 offen lds\n\t"
    "s_add_u32 s86, s86, 128\n\t"
    "s_cmp_eq_u32 s86, s46\n\t"
    "s_cselect_b32 s86, %[sx], s86\n\t"
    "s_waitcnt lgkmcnt(5)\n\t"
    "v_mfma_f32_32x32x16_bf16 v[0:15], v[184:187], v[144:147], v[0:15]\n\t"
    "s_add_u32 m0, %[ldw], 147456\n\t"
    "v_add_u32 v234, 32768, v234\n\t"
    "ds_read_b128 v[176:179], v234\n\t"
    "ds_read_b64_tr_b16 v[136:137], v220 offset:4096\n\t"
    "ds_read_b64_tr_b16 v[138:139], v220 offset:4608\n\t"
    "buffer_load_dwordx4 v217, %[rq], s87 offen lds\n\t"
    "s_add_u32 s87, s87, 172032\n\t"
    "s_cmp_eq_u32 s87, s47\n\t"
    "s_cselect_b32 s87, %[s2], s87\n\t"
    "v_mfma_f32_32x32x16_bf16 v[16:31], v[184:187], v[148:151], v[16:31]\n\t"
    "s_add_u32 m0, %[ldw], 155648\n\t"
    "ds_read_b64_tr_b16 v[140:141], v221 offset:4096\n\t"
    "ds_read_b64_tr_b16 v[142:143], v221 offset:4608\n\t"
    "buffer_load_dwordx4 v218, %[r3], s88 offen lds\n\t"
    "s_add_u32 s88, s88, %[st34]\n\t"
    "s_cmp_eq_u32 s88, s48\n\t"
    "s_cselect_b32 s88, s89, s88\n\t"
    "s_waitcnt lgkmcnt(5)\n\t"
    "v_mfma_f32_32x32x16_bf16 v[0:15], v[188:191], v[152:155], v[0:15]\n\t"
    "v_add_u32 v235, 32768, v235\n\t"
    "ds_read_b128 v[180:183], v235\n\t"
    "ds_read_b64_tr_b16 v[144:145], v220 offset:6144\n\t"
    "ds_read_b64_tr_b16 v[146:147], v220 offset:6656\n\t"
    "v_mfma_f32_32x32x16_bf16 v[16:31], v[188:191], v[156:159], v[16:31]\n\t"
    "s_add_u32 m0, %[ldw], 0\n\t"
    "ds_read_b64_tr_b16 v[148:149], v221 offset:6144\n\t"
    "ds_read_b64_tr_b16 v[150:151], v221 offset:6656\n\t"
    "buffer_load_dwordx4 v216, %[rx], s86 offen lds\n\t"
    "s_waitcnt vmcnt(9)\n\t"
    "s_waitcnt lgkmcnt(0)\n\t"
    "s_barrier\n\t"
    "v_mfma_f32_32x32x16_bf16 v[0:15], v[176:179], v[136:139], v[0:15]\n\t"
    "v_add_u32 v232, 32768, v232\n\t"
    "ds_read_b128 v[192:195], v232\n\t"
    "v_add_u32 v220, 32768, v220\n\t"
    "ds_read_b64_tr_b16 v[152:153], v220\n\t"
    "ds_read_b64_tr_b16 v[154:155], v220 offset:512\n\t"
    "v_mfma_f32_32x32x16_bf16 v[16:31], v[176:179], v[140:143], v[16:31]\n\t"
    "s_add_u32 m0, %[ldw], 8192\n\t"
    "v_add_u32 v221, 32768, v221\n\t"
    "ds_read_b64_tr_b16 v[156:157], v221\n\t"
    "ds_read_b64_tr_b16 v[158:159], v221 offset:512\n\t"
    "buffer_load_dwordx4 v241, %[rx], s86 offen lds\n\t"
    "s_add_u32 s86, s86, 128\n\t"
    "s_cmp_eq_u32 s86, s46\n\t"
    "s_cselect_b32 s86, %[sx], s86\n\t"
    "v_mfma_f32_32x32x16_bf16 v[0:15], v[180:183], v[144:147], v[0:15]\n\t"
    "v_add_u32 v233, 32768, v233\n\t"
    "ds_read_b128 v[196:199], v233\n\t"
    "ds_read_b64_tr_b16 v[136:137], v220 offset:2048\n\t"
    "ds_read_b64_tr_b16 v[138:139], v220 offset:2560\n\t"
    "v_mfma_f32_32x32x16_bf16 v[16:31], v[180:183], v[148:151], v[16:31]\n\t"
    "s_add_u32 m0, %[ldw], 16384\n\t"
    "ds_read_b64_tr_b16 v[140:141], v221 offset:2048\n\t"
    "ds_read_b64_tr_b16 v[142:143], v221 offset:2560\n\t"
    "buffer_load_dwordx4 v217, %[rq], s87 offen lds\n\t"
    "s_add_u32 s87, s87, 172032\n\t"
    "s_cmp_eq_u32 s87, s47\n\t"
    "s_cselect_b32 s87, %[s2], s87\n\t"
    "s_waitcnt lgkmcnt(5)\n\t"
    "v_mfma_f32_32x32x16_bf16 v[0:15], v[192:195], v[152:155], v[0:15]\n\t"
    "v_add_u32 v234, 32768, v234\n\t"
    "ds_read_b128 v[200:203], v234\n\t"
    "ds_read_b64_tr_b16 v[144:145], v220 offset:4096\n\t"
    "ds_read_b64_tr_b16 v[146:147], v220 offset:4608\n\t"
    "v_mfma_f32_32x32x16_bf16 v[16:31], v[192:195], v[156:159], v[16:31]\n\t"
    "s_add_u32 m0, %[ldw], 24576\n\t"
    "ds_read_b64_tr_b16 v[148:149], v221 offset:4096\n\t"
    "ds_read_b64_tr_b16 v[150:151], v221 offset:4608\n\t"
    "buffer_load_dwordx4 v218, %[r3], s88 offen lds\n\t"
    "s_add_u32 s88, s88, %[st34]\n\t"
    "s_cmp_eq_u32 s88, s48\n\t"
    "s_cselect_b32 s88, s89, s88\n\t"
    "s_waitcnt lgkmcnt(5)\n\t"
    "v_mfma_f32_32x32x16_bf16 v[0:15], v[196:199], v[136:139], v[0:15]\n\t"
    "v_add_u32 v235, 32768, v235\n\t"
    "ds_read_b128 v[204:207], v235\n\t"
    "ds_read_b64_tr_b16 v[152:153], v220 offset:6144\n\t"
    "ds_read_b64_tr_b16 v[154:155], v220 offset:6656\n\t"
    "v_mfma_f32_32x32x16_bf16 v[16:31], v[196:199], v[140:143], v[16:31]\n\t"
    "s_add_u32 m0, %[ldw], 32768\n\t"
    "ds_read_b64_tr_b16 v[156:157], v221 offset:6144\n\t"
    "ds_read_b64_tr_b16 v[158:159], v221 offset:6656\n\t"
    "buffer_load_dwordx4 v216, %[rx], s86 offen lds\n\t"
    "s_waitcnt vmcnt(9)\n\t"
    "s_waitcnt lgkmcnt(0)\n\t"
    "s_barrier\n\t"
    "v_mfma_f32_32x32x16_bf16 v[0:15], v[200:203], v[144:147], v[0:15]\n\t"
    "v_add_u32 v232, 32768, v232\n\t"
    "ds_read_b128 v[208:211], v232\n\t"
    "v_add_u32 v220, 32768, v220\n\t"
    "ds_read_b64_tr_b16 v[136:137], v220\n\t"
    "ds_read_b64_tr_b16 v[138:139], v220 offset:512\n\t"
    "v_mfma_f32_32x32x16_bf16 v[16:31], v[200:203], v[148:151], v[16:31]\n\t"
    "s_add_u32 m0, %[ldw], 40960\n\t"
    "v_add_u32 v221, 32768, v221\n\t"
    "ds_read_b64_tr_b16 v[140:141], v221\n\t"
    "ds_read_b64_tr_b16 v[142:143], v221 offset:512\n\t"
    "buffer_load_dwordx4 v241, %[rx], s86 offen lds\n\t"
    "s_add_u32 s86, s86, 128\n\t"
    "s_cmp_eq_u32 s86, s46\n\t"
    "s_cselect_b32 s86, %[sx], s86\n\t"
    "v_mfma_f32_32x32x16_bf16 v[0:15], v[204:207], v[152:155], v[0:15]\n\t"
    "s_add_u32 m0, %[ldw], 49152\n\t"
    "v_add_u32 v233, 32768, v233\n\t"
    "ds_read_b128 v[212:215], v233\n\t"
    "ds_read_b64_tr_b16 v[144:145], v220 offset:2048\n\t"
    "ds_read_b64_tr_b16 v[146:147], v220 offset:2560\n\t"
    "buffer_load_dwordx4 v217, %[rq], s87 offen lds\n\t"
    "s_add_u32 s87, s87, 172032\n\t"
    "s_cmp_eq_u32 s87, s47\n\t"
    "s_cselect_b32 s87, %[s2], s87\n\t"
    "v_mfma_f32_32x32x16_bf16 v[16:31], v[204:207], v[156:159], v[16:31]\n\t"
    "s_add_u32 m0, %[ldw], 57344\n\t"
    "ds_read_b64_tr_b16 v[148:149], v221 offset:2048\n\t"
    "ds_read_b64_tr_b16 v[150:151], v221 offset:2560\n\t"
    "buffer_load_dwordx4 v218, %[r3], s88 offen lds\n\t"
    "s_add_u32 s88, s88, %[st34]\n\t"
    "s_cmp_eq_u32 s88, s48\n\t"
    "s_cselect_b32 s88, s89, s88\n\t"
    "s_waitcnt lgkmcnt(5)\n\t"
    "v_mfma_f32_32x32x16_bf16 v[0:15], v[208:211], v[136:139], v[0:15]\n\t"
    "v_add_u32 v234, 32768, v234\n\t"
    "ds_read_b128 v[64:67], v234\n\t"
    "ds_read_b64_tr_b16 v[152:153], v220 offset:4096\n\t"
    "ds_read_b64_tr_b16 v[154:155], v220 offset:4608\n\t"
    "v_mfma_f32_32x32x16_bf16 v[16:31], v[208:211], v[140:143], v[16:31]\n\t"
    "s_add_u32 m0, %[ldw], 65536\n\t"
    "ds_read_b64_tr_b16 v[156:157], v221 offset:4096\n\t"
    "ds_read_b64_tr_b16 v[158:159], v221 offset:4608\n\t"
    "buffer_load_dwordx4 v219, %[r4], s88 offen lds\n\t"
    "s_add_u32 s88, s88, %[st34]\n\t"
    "s_cmp_eq_u32 s88, s48\n\t"
    "s_cselect_b32 s88, s89, s88\n\t"
    "s_waitcnt lgkmcnt(5)\n\t"
    "v_mfma_f32_32x32x16_bf16 v[0:15], v[212:215], v[144:147], v[0:15]\n\t"
    "v_add_u32 v235, 32768, v235\n\t"
    "ds_read_b128 v[68:71], v235\n\t"
    "ds_read_b64_tr_b16 v[136:137], v220 offset:6144\n\t"
    "ds_read_b64_tr_b16 v[138:139], v220 offset:6656\n\t"
    "v_mfma_f32_32x32x16_bf16 v[16:31], v[212:215], v[148:151], v[16:31]\n\t"
    "s_add_u32 m0, %[ldw], 73728\n\t"
    "ds_read_b64_tr_b16 v[140:141], v221 offset:6144\n\t"
    "ds_read_b64_tr_b16 v[142:143], v221 offset:6656\n\t"
    "buffer_load_dwordx4 v219, %[r4], s88 offen lds\n\t"
    "s_add_u32 s88, s88, %[st34]\n\t"
    "s_cmp_eq_u32 s88, s48\n\t"
    "s_cselect_b32 s88, s89, s88\n\t"
    "s_waitcnt vmcnt(10)\n\t"
    "s_waitcnt lgkmcnt(0)\n\t"
    "s_barrier\n\t"
    "v_mfma_f32_32x32x16_bf16 v[0:15], v[64:67], v[152:155], v[0:15]\n\t"
    "v_add_u32 v232, 32768, v232\n\t"
    "ds_read_b128 v[72:75], v232\n\t"
    "v_add_u32 v220, 32768, v220\n\t"
    "ds_read_b64_tr_b16 v[144:145], v220\n\t"
    "ds_read_b64_tr_b16 v[146:147], v220 offset:512\n\t"
    "v_mfma_f32_32x32x16_bf16 v[16:31], v[64:67], v[156:159], v[16:31]\n\t"
    "s_add_u32 m0, %[ldw], 81920\n\t"
    "v_add_u32 v221, 32768, v221\n\t"
    "ds_read_b64_tr_b16 v[148:149], v221\n\t"
    "ds_read_b64_tr_b16 v[150:151], v221 offset:512\n\t"
    "buffer_load_dwordx4 v219, %[r4], s88 offen lds\n\t"
    "s_add_u32 s88, s88, %[st34]\n\t"
    "s_cmp_eq_u32 s88, s48\n\t"
    "s_cselect_b32 s88, s89, s88\n\t"
    "v_mfma_f32_32x32x16_bf16 v[0:15], v[68:71], v[136:139], v[0:15]\n\t"
    "v_add_u32 v233, 32768, v233\n\t"
    "ds_read_b128 v[76:79], v233\n\t"
    "ds_read_b64_tr_b16 v[152:153], v220 offset:2048\n\t"
    "ds_read_b64_tr_b16 v[154:155], v220 offset:2560\n\t"
    "v_mfma_f32_32x32x16_bf16 v[16:31], v[68:71], v[140:143], v[16:31]\n\t"
    "s_add_u32 m0, %[ldw], 90112\n\t"
    "ds_read_b64_tr_b16 v[156:157], v221 offset:2048\n\t"
    "ds_read_b64_tr_b16 v[158:159], v221 offset:2560\n\t"
    "buffer_load_dwordx4 v219, %[r4], s88 offen lds\n\t"
    "s_add_u32 s88, s88, %[st34]\n\t"
    "s_cmp_eq_u32 s88, s48\n\t"
    "s_cselect_b32 s88, s89, s88\n\t"
    "s_waitcnt lgkmcnt(5)\n\t"
    "v_mfma_f32_32x32x16_bf16 v[0:15], v[72:75], v[144:147], v[0:15]\n\t"
    "v_add_u32 v234, 32768, v234\n\t"
    "ds_read_b128 v[80:83], v234\n\t"
    "ds_read_b64_tr_b16 v[136:137], v220 offset:4096\n\t"
    "ds_read_b64_tr_b16 v[138:139], v220 offset:4608\n\t"
    "v_mfma_f32_32x32x16_bf16 v[16:31], v[72:75], v[148:151], v[16:31]\n\t"
    "s_add_u32 m0, %[ldw], 98304\n\t"
    "ds_read_b64_tr_b16 v[140:141], v221 offset:4096\n\t"
    "ds_read_b64_tr_b16 v[142:143], v221 offset:4608\n\t"
    "buffer_load_dwordx4 v219, %[r4], s88 offen lds\n\t"
    "s_add_u32 s88, s88, %[st34]\n\t"
    "s_cmp_eq_u32 s88, s48\n\t"
    "s_cselect_b32 s88, s89, s88\n\t"
    "s_waitcnt lgkmcnt(5)\n\t"
    "v_mfma_f32_32x32x16_bf16 v[0:15], v[76:79], v[152:155], v[0:15]\n\t"
    "v_add_u32 v235, 32768, v235\n\t"
    "ds_read_b128 v[84:87], v235\n\t"
    "ds_read_b64_tr_b16 v[144:145], v220 offset:6144\n\t"
    "ds_read_b64_tr_b16 v[146:147], v220 offset:6656\n\t"
    "v_mfma_f32_32x32x16_bf16 v[16:31], v[76:79], v[156:159], v[16:31]\n\t"
    "s_add_u32 m0, %[ldw], 106496\n\t"
    "ds_read_b64_tr_b16 v[148:149], v221 offset:6144\n\t"
    "ds_read_b64_tr_b16 v[150:151], v221 offset:6656\n\t"
    "buffer_load_dwordx4 v219, %[r4], s88 offen lds\n\t"
    "s_add_u32 s88, s88, %[st34]\n\t"
    "s_cmp_eq_u32 s88, s48\n\t"
    "s_cselect_b32 s88, s89, s88\n\t"
    "s_waitcnt vmcnt(10)\n\t"
    "s_waitcnt lgkmcnt(0)\n\t"
    "s_barrier\n\t"
    "v_mfma_f32_32x32x16_bf16 v[0:15], v[80:83], v[136:139], v[0:15]\n\t"
    "v_add_u32 v232, -131072, v232\n\t"
    "ds_read_b128 v[88:91], v232\n\t"
    "v_add_u32 v220, -131072, v220\n\t"
    "ds_read_b64_tr_b16 v[152:153], v220\n\t"
    "ds_read_b64_tr_b16 v[154:155], v220 offset:512\n\t"
    "v_mfma_f32_32x32x16_bf16 v[16:31], v[80:83], v[140:143], v[16:31]\n\t"
    "s_add_u32 m0, %[ldw], 114688\n\t"
    "v_add_u32 v221, -131072, v221\n\t"
    "ds_read_b64_tr_b16 v[156:157], v221\n\t"
    "ds_read_b64_tr_b16 v[158:159], v221 offset:512\n\t"
    "buffer_load_dwordx4 v219, %[r4], s88 offen lds\n\t"
    "s_add_u32 s88, s88, %[st34]\n\t"
    "s_cmp_eq_u32 s88, s48\n\t"
    "s_cselect_b32 s88, s89, s88\n\t"
    "s_add_u32 s88, s88, s74\n\t"
    "s_add_u32 s48, s48, s74\n\t"
    "s_add_u32 s89, s89, s74\n\t"
    "v_mfma_f32_32x32x16_bf16 v[0:15], v[84:87], v[144:147], v[0:15]\n\t"
    "v_add_u32 v233, -131072, v233\n\t"
    "ds_read_b128 v[92:95], v233\n\t"
    "ds_read_b64_tr_b16 v[136:137], v220 offset:2048\n\t"
    "ds_read_b64_tr_b16 v[138:139], v220 offset:2560\n\t"
    "v_mfma_f32_32x32x16_bf16 v[16:31], v[84:87], v[148:151], v[16:31]\n\t"
    "ds_read_b64_tr_b16 v[140:141], v221 offset:2048\n\t"
    "ds_read_b64_tr_b16 v[142:143], v221 offset:2560\n\t"
    "s_waitcnt lgkmcnt(5)\n\t"
    "v_mfma_f32_32x32x16_bf16 v[0:15], v[88:91], v[152:155], v[0:15]\n\t"
    "s_add_u32 m0, %[ldw], 122880\n\t"
    "v_add_u32 v234, -131072, v234\n\t"
    "ds_read_b128 v[96:99], v234\n\t"
    "ds_read_b64_tr_b16 v[144:145], v220 offset:4096\n\t"
    "ds_read_b64_tr_b16 v[146:147], v220 offset:4608\n\t"
    "buffer_load_dwordx4 v218, %[r3], s88 offen lds\n\t"
    "s_add_u32 s88, s88, %[st34]\n\t"
    "s_cmp_eq_u32 s88, s48\n\t"
    "s_cselect_b32 s88, s89, s88\n\t"
    "v_mfma_f32_32x32x16_bf16 v[16:31], v[88:91], v[156:159], v[16:31]\n\t"
    "ds_read_b64_tr_b16 v[148:149], v221 offset:4096\n\t"
    "ds_read_b64_tr_b16 v[150:151], v221 offset:4608\n\t"
    "s_waitcnt lgkmcnt(5)\n\t"
    "v_mfma_f32_32x32x16_bf16 v[0:15], v[92:95], v[136:139], v[0:15]\n\t"
    "s_add_u32 m0, %[ldw], 131072\n\t"
    "v_add_u32 v235, -131072, v235\n\t"
    "ds_read_b128 v[100:103], v235\n\t"
    "ds_read_b64_tr_b16 v[152:153], v220 offset:6144\n\t"
    "ds_read_b64_tr_b16 v[154:155], v220 offset:6656\n\t"
    "buffer_load_dwordx4 v218, %[r3], s88 offen lds\n\t"
    "s_add_u32 s88, s88, %[st34]\n\t"
    "s_cmp_eq_u32 s88, s48\n\t"
    "s_cselect_b32 s88, s89, s88\n\t"
    "v_mfma_f32_32x32x16_bf16 v[16:31], v[92:95], v[140:143], v[16:31]\n\t"
    "ds_read_b64_tr_b16 v[156:157], v221 offset:6144\n\t"
    "ds_read_b64_tr_b16 v[158:159], v221 offset:6656\n\t"
    "s_waitcnt vmcnt(9)\n\t"
    "s_waitcnt lgkmcnt(0)\n\t"
    "s_barrier\n\t"
    "v_mfma_f32_32x32x16_bf16 v[0:15], v[96:99], v[144:147], v[0:15]\n\t"
    "s_add_u32 m0, %[ldw], 139264\n\t"
    "v_add_u32 v232, 32768, v232\n\t"
    "ds_read_b128 v[104:107], v232\n\t"
    "v_add_u32 v220, 32768, v220\n\t"
    "ds_read_b64_tr_b16 v[136:137], v220\n\t"
    "ds_read_b64_tr_b16 v[138:139], v220 offset:512\n\t"
    "buffer_load_dwordx4 v218, %[r3], s88 offen lds\n\t"
    "s_add_u32 s88, s88, %[st34]\n\t"
    "s_cmp_eq_u32 s88, s48\n\t"
    "s_cselect_b32 s88, s89, s88\n\t"
    "v_mfma_f32_32x32x16_bf16 v[16:31], v[96:99], v[148:151], v[16:31]\n\t"
    "v_add_u32 v221, 32768, v221\n\t"
    "ds_read_b64_tr_b16 v[140:141], v221\n\t"
    "ds_read_b64_tr_b16 v[142:143], v221 offset:512\n\t"
    "v_mfma_f32_32x32x16_bf16 v[0:15], v[100:103], v[152:155], v[0:15]\n\t"
    "s_add_u32 m0, %[ldw], 147456\n\t"
    "v_add_u32 v233, 32768, v233\n\t"
    "ds_read_b128 v[108:111], v233\n\t"
    "ds_read_b64_tr_b16 v[144:145], v220 offset:2048\n\t"
    "ds_read_b64_tr_b16 v[146:147], v220 offset:2560\n\t"
    "buffer_load_dwordx4 v218, %[r3], s88 offen lds\n\t"
    "s_add_u32 s88, s88, %[st34]\n\t"
    "s_cmp_eq_u32 s88, s48\n\t"
    "s_cselect_b32 s88, s89, s88\n\t"
    "v_mfma_f32_32x32x16_bf16 v[16:31], v[100:103], v[156:159], v[16:31]\n\t"
    "ds_read_b64_tr_b16 v[148:149], v221 offset:2048\n\t"
    "ds_read_b64_tr_b16 v[150:151], v221 offset:2560\n\t"
    "s_waitcnt lgkmcnt(5)\n\t"
    "v_mfma_f32_32x32x16_bf16 v[0:15], v[104:107], v[136:139], v[0:15]\n\t"
    "s_add_u32 m0, %[ldw], 155648\n\t"
    "v_add_u32 v234, 32768, v234\n\t"
    "ds_read_b128 v[112:115], v234\n\t"
    "ds_read_b64_tr_b16 v[152:153], v220 offset:4096\n\t"
    "ds_read_b64_tr_b16 v[154:155], v220 offset:4608\n\t"
    "buffer_load_dwordx4 v218, %[r3], s88 offen lds\n\t"
    "s_add_u32 s88, s88, %[st34]\n\t"
    "s_cmp_eq_u32 s88, s48\n\t"
    "s_cselect_b32 s88, s89, s88\n\t"
    "v_mfma_f32_32x32x16_bf16 v[16:31], v[104:107], v[140:143], v[16:31]\n\t"
    "ds_read_b64_tr_b16 v[156:157], v221 offset:4096\n\t"
    "ds_read_b64_tr_b16 v[158:159], v221 offset:4608\n\t"
    "s_waitcnt lgkmcnt(5)\n\t"
    "v_mfma_f32_32x32x16_bf16 v[0:15], v[108:111], v[144:147], v[0:15]\n\t"
    "s_add_u32 m0, %[ldw], 0\n\t"
    "v_add_u32 v235, 32768, v235\n\t"
    "ds_read_b128 v[116:119], v235\n\t"
    "ds_read_b64_tr_b16 v[136:137], v220 offset:6144\n\t"
    "ds_read_b64_tr_b16 v[138:139], v220 offset:6656\n\t"
    "buffer_load_dwordx4 v218, %[r3], s88 offen lds\n\t"
    "s_add_u32 s88, s88, %[st34]\n\t"
    "s_cmp_eq_u32 s88, s48\n\t"
    "s_cselect_b32 s88, s89, s88\n\t"
    "v_mfma_f32_32x32x16_bf16 v[16:31], v[108:111], v[148:151], v[16:31]\n\t"
    "ds_read_b64_tr_b16 v[140:141], v221 offset:6144\n\t"
    "ds_read_b64_tr_b16 v[142:143], v221 offset:6656\n\t"
    "s_waitcnt vmcnt(6)\n\t"
    "s_waitcnt lgkmcnt(0)\n\t"
    "s_barrier\n\t"
    "v_mfma_f32_32x32x16_bf16 v[0:15], v[112:115], v[152:155], v[0:15]\n\t"
    "s_add_u32 m0, %[ldw], 8192\n\t"
    "v_add_u32 v221, 8192, v221\n\t"
    "ds_read_b64_tr_b16 v[144:145], v221\n\t"
    "ds_read_b64_tr_b16 v[146:147], v221 offset:512\n\t"
    "buffer_load_dwordx4 v218, %[r3], s88 offen lds\n\t"
    "s_add_u32 s88, s88, %[st34]\n\t"
    "s_cmp_eq_u32 s88, s48\n\t"
    "s_cselect_b32 s88, s89, s88\n\t"
    "v_mfma_f32_32x32x16_bf16 v[16:31], v[112:115], v[156:159], v[16:31]\n\t"
    "ds_read_b64_tr_b16 v[148:149], v221 offset:2048\n\t"
    "ds_read_b64_tr_b16 v[150:151], v221 offset:2560\n\t"
    "v_mfma_f32_32x32x16_bf16 v[0:15], v[116:119], v[136:139], v[0:15]\n\t"
    "ds_read_b64_tr_b16 v[152:153], v221 offset:4096\n\t"
    "ds_read_b64_tr_b16 v[154:155], v221 offset:4608\n\t"
    "v_mfma_f32_32x32x16_bf16 v[16:31], v[116:119], v[140:143], v[16:31]\n\t"
    "ds_read_b64_tr_b16 v[156:157], v221 offset:6144\n\t"
    "ds_read_b64_tr_b16 v[158:159], v221 offset:6656\n\t"
    "buffer_store_dwordx4 v[160:163], v236, %[rw], 0 offen sc1\n\t"
    "s_waitcnt lgkmcnt(6)\n\t"
    "v_mfma_f32_32x32x16_bf16 v[32:47], v[168:171], v[144:147], 0\n\t"
    "v_add_u32 v221, 8192, v221\n\t"
    "ds_read_b64_tr_b16 v[136:137], v221\n\t"
    "ds_read_b64_tr_b16 v[138:139], v221 offset:512\n\t"
    "s_add_u32 m0, %[ldw], 16384\n\t"
    "s_mov_b32 exec_hi, 0xffffff\n\t"
    "global_load_lds_dwordx4 v252, %[gp]\n\t"
    "s_mov_b64 exec, -1\n\t"
    "s_waitcnt lgkmcnt(6)\n\t"
    "v_mfma_f32_32x32x16_bf16 v[32:47], v[172:175], v[148:151], v[32:47]\n\t"
    "ds_read_b64_tr_b16 v[140:141], v221 offset:2048\n\t"
    "ds_read_b64_tr_b16 v[142:143], v221 offset:2560\n\t"
    "s_waitcnt lgkmcnt(6)\n\t"
    "v_mfma_f32_32x32x16_bf16 v[32:47], v[224:227], v[152:155], v[32:47]\n\t"
    "ds_read_b64_tr_b16 v[144:145], v221 offset:4096\n\t"
    "ds_read_b64_tr_b16 v[146:147], v221 offset:4608\n\t"
    "s_add_u32 s81, s72, 0\n\t"
    "v_add_u32 v236, s81, v242\n\t"
    "v_mul_u32_u24 v237, 2341, v236\n\t"
    "v_lshrrev_b32 v237, 16, v237\n\t"
    "v_mul_u32_u24 v238, 28, v237\n\t"
    "v_sub_u32 v236, v236, v238\n\t"
    "v_lshl_add_u32 v237, v237, 18, v243\n\t"
    "v_lshl_add_u32 v236, v236, 4, v237\n\t"
    "v_cvt_pk_bf16_f32 v160, v0, v1\n\t"
    "s_waitcnt lgkmcnt(6)\n\t"
    "v_mfma_f32_32x32x16_bf16 v[32:47], v[228:231], v[156:159], v[32:47]\n\t"
    "ds_read_b64_tr_b16 v[148:149], v221 offset:6144\n\t"
    "ds_read_b64_tr_b16 v[150:151], v221 offset:6656\n\t"
    "v_cvt_pk_bf16_f32 v161, v2, v3\n\t"
    "v_cvt_pk_bf16_f32 v162, v4, v5\n\t"
    "v_cvt_pk_bf16_f32 v163, v6, v7\n\t"
    "buffer_store_dwordx4 v[160:163], v236, %[rw], 0 offen sc1\n\t"
    "v_cvt_pk_bf16_f32 v164, v8, v9\n\t"
    "v_cvt_pk_bf16_f32 v165, v10, v11\n\t"
    "s_waitcnt lgkmcnt(6)\n\t"
    "v_mfma_f32_32x32x16_bf16 v[32:47], v[184:187], v[136:139], v[32:47]\n\t"
    "v_add_u32 v221, 8192, v221\n\t"
    "ds_read_b64_tr_b16 v[152:153], v221\n\t"
    "ds_read_b64_tr_b16 v[154:155], v221 offset:512\n\t"
    "v_cvt_pk_bf16_f32 v166, v12, v13\n\t"
    "v_cvt_pk_bf16_f32 v167, v14, v15\n\t"
    "buffer_store_dwordx4 v[164:167], v236, %[rw], 0 offen offset:1024 sc1\n\t"
    "s_add_u32 s81, s72, 64\n\t"
    "s_nop 0\n\t"
    "v_add_u32 v236, s81, v242\n\t"
    "v_mul_u32_u24 v237, 2341, v236\n\t"
    "v_lshrrev_b32 v237, 16, v237\n\t"
    "s_waitcnt lgkmcnt(6)\n\t"
    "v_mfma_f32_32x32x16_bf16 v[32:47], v[188:191], v[140:143], v[32:47]\n\t"
    "ds_read_b64_tr_b16 v[156:157], v221 offset:2048\n\t"
    "ds_read_b64_tr_b16 v[158:159], v221 offset:2560\n\t"
    "v_mul_u32_u24 v238, 28, v237\n\t"
    "v_sub_u32 v236, v236, v238\n\t"
    "v_lshl_add_u32 v237, v237, 18, v243\n\t"
    "v_lshl_add_u32 v236, v236, 4, v237\n\t"
    "v_cvt_pk_bf16_f32 v160, v16, v17\n\t"
    "v_cvt_pk_bf16_f32 v161, v18, v19\n\t"
    "v_cvt_pk_bf16_f32 v162, v20, v21\n\t"
    "v_cvt_pk_bf16_f32 v163, v22, v23\n\t"
    "s_waitcnt lgkmcnt(6)\n\t"
    "v_mfma_f32_32x32x16_bf16 v[32:47], v[176:179], v[144:147], v[32:47]\n\t"
    "ds_read_b64_tr_b16 v[136:137], v221 offset:4096\n\t"
    "ds_read_b64_tr_b16 v[138:139], v221 offset:4608\n\t"
    "buffer_store_dwordx4 v[160:163], v236, %[rw], 0 offen sc1\n\t"
    "v_cvt_pk_bf16_f32 v164, v24, v25\n\t"
    "v_cvt_pk_bf16_f32 v165, v26, v27\n\t"
    "v_cvt_pk_bf16_f32 v166, v28, v29\n\t"
    "v_cvt_pk_bf16_f32 v167, v30, v31\n\t"
    "buffer_store_dwordx4 v[164:167], v236, %[rw], 0 offen offset:1024 sc1\n\t"
    "s_waitcnt lgkmcnt(6)\n\t"
    "v_mfma_f32_32x32x16_bf16 v[32:47], v[180:183], v[148:151], v[32:47]\n\t"
    "ds_read_b64_tr_b16 v[140:141], v221 offset:6144\n\t"
    "ds_read_b64_tr_b16 v[142:143], v221 offset:6656\n\t"
    "s_waitcnt lgkmcnt(6)\n\t"
    "v_mfma_f32_32x32x16_bf16 v[32:47], v[192:195], v[152:155], v[32:47]\n\t"
    "v_add_u32 v221, 8192, v221\n\t"
    "ds_read_b64_tr_b16 v[144:145], v221\n\t"
    "ds_read_b64_tr_b16 v[146:147], v221 offset:512\n\t"
    "s_waitcnt lgkmcnt(6)\n\t"
    "v_mfma_f32_32x32x16_bf16 v[32:47], v[196:199], v[156:159], v[32:47]\n\t"
    "ds_read_b64_tr_b16 v[148:149], v221 offset:2048\n\t"
    "ds_read_b64_tr_b16 v[150:151], v221 offset:2560\n\t"
    "s_waitcnt lgkmcnt(6)\n\t"
    "v_mfma_f32_32x32x16_bf16 v[32:47], v[200:203], v[136:139], v[32:47]\n\t"
    "ds_read_b64_tr_b16 v[152:153], v221 offset:4096\n\t"
    "ds_read_b64_tr_b16 v[154:155], v221 offset:4608\n\t"
    "s_waitcnt lgkmcnt(6)\n\t"
    "v_mfma_f32_32x32x16_bf16 v[32:47], v[204:207], v[140:143], v[32:47]\n\t"
    "ds_read_b64_tr_b16 v[156:157], v221 offset:6144\n\t"
    "ds_read_b64_tr_b16 v[158:159], v221 offset:6656\n\t"
    "s_waitcnt lgkmcnt(6)\n\t"
    "v_mfma_f32_32x32x16_bf16 v[32:47], v[208:211], v[144:147], v[32:47]\n\t"
    "v_add_u32 v221, 8192, v221\n\t"
    "ds_read_b64_tr_b16 v[136:137], v221\n\t"
    "ds_read_b64_tr_b16 v[138:139], v221 offset:512\n\t"
    "s_waitcnt lgkmcnt(6)\n\t"
    "v_mfma_f32_32x32x16_bf16 v[32:47], v[212:215], v[148:151], v[32:47]\n\t"
    "ds_read_b64_tr_b16 v[140:141], v221 offset:2048\n\t"
    "ds_read_b64_tr_b16 v[142:143], v221 offset:2560\n\t"
    "s_waitcnt lgkmcnt(6)\n\t"
    "v_mfma_f32_32x32x16_bf16 v[32:47], v[64:67], v[152:155], v[32:47]\n\t"
    "ds_read_b64_tr_b16 v[144:145], v221 offset:4096\n\t"
    "ds_read_b64_tr_b16 v[146:147], v221 offset:4608\n\t"
    "s_waitcnt lgkmcnt(6)\n\t"
    "v_mfma_f32_32x32x16_bf16 v[32:47], v[68:71], v[156:159], v[32:47]\n\t"
    "ds_read_b64_tr_b16 v[148:149], v221 offset:6144\n\t"
    "ds_read_b64_tr_b16 v[150:151], v221 offset:6656\n\t"
    "s_waitcnt lgkmcnt(6)\n\t"
    "v_mfma_f32_32x32x16_bf16 v[32:47], v[72:75], v[136:139], v[32:47]\n\t"
    "v_add_u32 v221, 8192, v221\n\t"
    "ds_read_b64_tr_b16 v[152:153], v221\n\t"
    "ds_read_b64_tr_b16 v[154:155], v221 offset:512\n\t"
    "s_waitcnt lgkmcnt(6)\n\t"
    "v_mfma_f32_32x32x16_bf16 v[32:47], v[76:79], v[140:143], v[32:47]\n\t"
    "ds_read_b64_tr_b16 v[156:157], v221 offset:2048\n\t"
    "ds_read_b64_tr_b16 v[158:159], v221 offset:2560\n\t"
    "s_waitcnt lgkmcnt(6)\n\t"
    "v_mfma_f32_32x32x16_bf16 v[32:47], v[80:83], v[144:147], v[32:47]\n\t"
    "ds_read_b64_tr_b16 v[136:137], v221 offset:4096\n\t"
    "ds_read_b64_tr_b16 v[138:139], v221 offset:4608\n\t"
    "s_waitcnt lgkmcnt(6)\n\t"
    "v_mfma_f32_32x32x16_bf16 v[32:47], v[84:87], v[148:151], v[32:47]\n\t"
    "ds_read_b64_tr_b16 v[140:141], v221 offset:6144\n\t"
    "ds_read_b64_tr_b16 v[142:143], v221 offset:6656\n\t"
    "s_waitcnt lgkmcnt(6)\n\t"
    "v_mfma_f32_32x32x16_bf16 v[32:47], v[88:91], v[152:155], v[32:47]\n\t"
    "v_add_u32 v221, 8192, v221\n\t"
    "ds_read_b64_tr_b16 v[144:145], v221\n\t"
    "ds_read_b64_tr_b16 v[146:147], v221 offset:512\n\t"
    "s_waitcnt lgkmcnt(6)\n\t"
    "v_mfma_f32_32x32x16_bf16 v[32:47], v[92:95], v[156:159], v[32:47]\n\t"
    "ds_read_b64_tr_b16 v[148:149], v221 offset:2048\n\t"
    "ds_read_b64_tr_b16 v[150:151], v221 offset:2560\n\t"
    "s_waitcnt lgkmcnt(6)\n\t"
    "v_mfma_f32_32x32x16_bf16 v[32:47], v[96:99], v[136:139], v[32:47]\n\t"
    "ds_read_b64_tr_b16 v[152:153], v221 offset:4096\n\t"
    "ds_read_b64_tr_b16 v[154:155], v221 offset:4608\n\t"
    "s_waitcnt lgkmcnt(6)\n\t"
    "v_mfma_f32_32x32x16_bf16 v[32:47], v[100:103], v[140:143], v[32:47]\n\t"
    "ds_read_b64_tr_b16 v[156:157], v221 offset:6144\n\t"
    "ds_read_b64_tr_b16 v[158:159], v221 offset:6656\n\t"
    "s_waitcnt vmcnt(6)\n\t"
    "s_barrier\n\t"
    "v_readfirstlane_b32 s84, v120\n\t"
    "v_readfirstlane_b32 s85, v121\n\t"
    "s_nop 0\n\t"
    "s_add_u32 s84, s84, 1\n\t"
    "s_addc_u32 s85, s85, 0\n\t"
    "v_lshlrev_b32 v241, 3, v252\n\t"
    "s_mov_b64 exec, 0xff\n\t"
    "s_lshl_b32 s80, %[bid], 13\n\t"
    "s_add_u32 s80, s80, %[ldw]\n\t"
    "s_cmp_lt_u32 s80, 1605632\n\t"
    "s_cbranch_scc1 Ltdq_%=\n\t"
    "s_add_u32 s80, s80, -1605632\n\t"
    "buffer_load_dword v241, v241, %[ro], s80 offen\n\t"
    "s_branch Lteq_%=\n\t"
    "Ltdq_%=:\n\t"
    "buffer_load_dword v241, v241, %[rd], s80 offen\n\t"
    "Lteq_%=:\n\t"
    "s_mov_b64 exec, -1\n\t"
    "s_waitcnt lgkmcnt(6)\n\t"
    "v_mfma_f32_32x32x16_bf16 v[32:47], v[104:107], v[144:147], v[32:47]\n\t"
    "v_add_u32 v221, 8192, v221\n\t"
    "ds_read_b64_tr_b16 v[136:137], v221\n\t"
    "ds_read_b64_tr_b16 v[138:139], v221 offset:512\n\t"
    "s_waitcnt lgkmcnt(6)\n\t"
    "v_mfma_f32_32x32x16_bf16 v[32:47], v[108:111], v[148:151], v[32:47]\n\t"
    "ds_read_b64_tr_b16 v[140:141], v221 offset:2048\n\t"
    "ds_read_b64_tr_b16 v[142:143], v221 offset:2560\n\t"
    "s_waitcnt lgkmcnt(6)\n\t"
    "v_mfma_f32_32x32x16_bf16 v[32:47], v[112:115], v[152:155], v[32:47]\n\t"
    "ds_read_b64_tr_b16 v[144:145], v221 offset:4096\n\t"
    "ds_read_b64_tr_b16 v[146:147], v221 offset:4608\n\t"
    "s_waitcnt lgkmcnt(6)\n\t"
    "v_mfma_f32_32x32x16_bf16 v[32:47], v[116:119], v[156:159], v[32:47]\n\t"
    "ds_read_b64_tr_b16 v[148:149], v221 offset:6144\n\t"
    "ds_read_b64_tr_b16 v[150:151], v221 offset:6656\n\t"
    "s_bitcmp1_b32 %[ty], 2\n\t"
    "s_cbranch_scc1 Lduty_%=\n\t"
    "s_add_u32 s81, s72, 128\n\t"
    "v_add_u32 v236, s81, v242\n\t"
    "v_mul_u32_u24 v237, 2341, v236\n\t"
    "v_lshrrev_b32 v237, 16, v237\n\t"
    "v_mul_u32_u24 v238, 28, v237\n\t"
    "v_sub_u32 v236, v236, v238\n\t"
    "v_lshl_add_u32 v237, v237, 18, v243\n\t"
    "v_lshl_add_u32 v236, v236, 4, v237\n\t"
    "v_cvt_pk_bf16_f32 v160, v32, v33\n\t"
    "v_cvt_pk_bf16_f32 v161, v34, v35\n\t"
    "v_cvt_pk_bf16_f32 v162, v36, v37\n\t"
    "v_cvt_pk_bf16_f32 v163, v38, v39\n\t"
    "buffer_store_dwordx4 v[160:163], v236, %[rw], 0 offen sc1\n\t"
    "v_cvt_pk_bf16_f32 v164, v40, v41\n\t"
    "v_cvt_pk_bf16_f32 v165, v42, v43\n\t"
    "v_cvt_pk_bf16_f32 v166, v44, v45\n\t"
    "v_cvt_pk_bf16_f32 v167, v46, v47\n\t"
    "buffer_store_dwordx4 v[164:167], v236, %[rw], 0 offen offset:1024 sc1\n\t"
    "s_bitcmp1_b32 %[ty], 3\n\t"
    "s_cbranch_scc0 Lqdone_%=\n\t"
    "s_waitcnt vmcnt(7)\n\t"
    "s_lshl_b32 s86, %[rot], 7\n\t"
    "s_mov_b32 s89, %[rot]\n\t"
    "s_add_u32 s80, %[ldw], 16384\n\t"
    "v_lshl_add_u32 v14, v244, 4, s80\n\t"
    "v_add_u32 v15, s86, v14\n\t"
    "s_add_u32 s89, s89, 1\n\t"
    "s_add_u32 s86, s86, 128\n\t"
    "s_cmp_eq_u32 s89, 7\n\t"
    "s_cselect_b32 s89, 0, s89\n\t"
    "s_cselect_b32 s86, 0, s86\n\t"
    "ds_read_b128 v[16:19], v15\n\t"
    "ds_read_b128 v[20:23], v15 offset:32\n\t"
    "ds_read_b128 v[24:27], v15 offset:64\n\t"
    "ds_read_b128 v[28:31], v15 offset:96\n\t"
    "v_add_u32 v15, s86, v14\n\t"
    "s_add_u32 s89, s89, 1\n\t"
    "s_add_u32 s86, s86, 128\n\t"
    "s_cmp_eq_u32 s89, 7\n\t"
    "s_cselect_b32 s89, 0, s89\n\t"
    "s_cselect_b32 s86, 0, s86\n\t"
    "ds_read_b128 v[32:35], v15\n\t"
    "ds_read_b128 v[36:39], v15 offset:32\n\t"
    "ds_read_b128 v[40:43], v15 offset:64\n\t"
    "ds_read_b128 v[44:47], v15 offset:96\n\t"
    "s_waitcnt lgkmcnt(7)\n\t"
    "v_mfma_f32_32x32x16_bf16 v[120:135], v[16:19], v[168:171], 0\n\t"
    "v_add_u32 v15, s86, v14\n\t"
    "s_add_u32 s89, s89, 1\n\t"
    "s_add_u32 s86, s86, 128\n\t"
    "s_cmp_eq_u32 s89, 7\n\t"
    "s_cselect_b32 s89, 0, s89\n\t"
    "s_cselect_b32 s86, 0, s86\n\t"
    "ds_read_b128 v[48:51], v15\n\t"
    "s_waitcnt lgkmcnt(7)\n\t"
    "v_mfma_f32_32x32x16_bf16 v[120:135], v[20:23], v[172:175], v[120:135]\n\t"
    "ds_read_b128 v[52:55], v15 offset:32\n\t"
    "s_waitcnt lgkmcnt(7)\n\t"
    "v_mfma_f32_32x32x16_bf16 v[120:135], v[24:27], v[224:227], v[120:135]\n\t"
    "ds_read_b128 v[56:59], v15 offset:64\n\t"
    "s_waitcnt lgkmcnt(7)\n\t"
    "v_mfma_f32_32x32x16_bf16 v[120:135], v[28:31], v[228:231], v[120:135]\n\t"
    "ds_read_b128 v[60:63], v15 offset:96\n\t"
    "s_waitcnt lgkmcnt(7)\n\t"
    "v_mfma_f32_32x32x16_bf16 v[120:135], v[32:35], v[184:187], v[120:135]\n\t"
    "v_add_u32 v15, s86, v14\n\t"
    "s_add_u32 s89, s89, 1\n\t"
    "s_add_u32 s86, s86, 128\n\t"
    "s_cmp_eq_u32 s89, 7\n\t"
    "s_cselect_b32 s89, 0, s89\n\t"
    "s_cselect_b32 s86, 0, s86\n\t"
    "ds_read_b128 v[16:19], v15\n\t"
    "s_waitcnt lgkmcnt(7)\n\t"
    "v_mfma_f32_32x32x16_bf16 v[120:135], v[36:39], v[188:191], v[120:135]\n\t"
    "ds_read_b128 v[20:23], v15 offset:32\n\t"
    "s_waitcnt lgkmcnt(7)\n\t"
    "v_mfma_f32_32x32x16_bf16 v[120:135], v[40:43], v[176:179], v[120:135]\n\t"
    "ds_read_b128 v[24:27], v15 offset:64\n\t"
    "s_waitcnt lgkmcnt(7)\n\t"
    "v_mfma_f32_32x32x16_bf16 v[120:135], v[44:47], v[180:183], v[120:135]\n\t"
    "ds_read_b128 v[28:31], v15 offset:96\n\t"
    "s_waitcnt lgkmcnt(7)\n\t"
    "v_mfma_f32_32x32x16_bf16 v[120:135], v[48:51], v[192:195], v[120:135]\n\t"
    "v_add_u32 v15, s86, v14\n\t"
    "s_add_u32 s89, s89, 1\n\t"
    "s_add_u32 s86, s86, 128\n\t"
    "s_cmp_eq_u32 s89, 7\n\t"
    "s_cselect_b32 s89, 0, s89\n\t"
    "s_cselect_b32 s86, 0, s86\n\t"
    "ds_read_b128 v[32:35], v15\n\t"
    "s_waitcnt lgkmcnt(7)\n\t"
    "v_mfma_f32_32x32x16_bf16 v[120:135], v[52:55], v[196:199], v[120:135]\n\t"
    "ds_read_b128 v[36:39], v15 offset:32\n\t"
    "s_waitcnt lgkmcnt(7)\n\t"
    "v_mfma_f32_32x32x16_bf16 v[120:135], v[56:59], v[200:203], v[120:135]\n\t"
    "ds_read_b128 v[40:43], v15 offset:64\n\t"
    "s_waitcnt lgkmcnt(7)\n\t"
    "v_mfma_f32_32x32x16_bf16 v[120:135], v[60:63], v[204:207], v[120:135]\n\t"
    "ds_read_b128 v[44:47], v15 offset:96\n\t"
    "s_waitcnt lgkmcnt(7)\n\t"
    "v_mfma_f32_32x32x16_bf16 v[120:135], v[16:19], v[208:211], v[120:135]\n\t"
    "v_add_u32 v15, s86, v14\n\t"
    "s_add_u32 s89, s89, 1\n\t"
    "s_add_u32 s86, s86, 128\n\t"
    "s_cmp_eq_u32 s89, 7\n\t"
    "s_cselect_b32 s89, 0, s89\n\t"
    "s_cselect_b32 s86, 0, s86\n\t"
    "ds_read_b128 v[48:51], v15\n\t"
    "s_waitcnt lgkmcnt(7)\n\t"
    "v_mfma_f32_32x32x16_bf16 v[120:135], v[20:23], v[212:215], v[120:135]\n\t"
    "ds_read_b128 v[52:55], v15 offset:32\n\t"
    "s_waitcnt lgkmcnt(7)\n\t"
    "v_mfma_f32_32x32x16_bf16 v[120:135], v[24:27], v[64:67], v[120:135]\n\t"
    "ds_read_b128 v[56:59], v15 offset:64\n\t"
    "s_waitcnt lgkmcnt(7)\n\t"
    "v_mfma_f32_32x32x16_bf16 v[120:135], v[28:31], v[68:71], v[120:135]\n\t"
    "ds_read_b128 v[60:63], v15 offset:96\n\t"
    "s_waitcnt lgkmcnt(7)\n\t"
    "v_mfma_f32_32x32x16_bf16 v[120:135], v[32:35], v[72:75], v[120:135]\n\t"
    "v_add_u32 v15, s86, v14\n\t"
    "s_add_u32 s89, s89, 1\n\t"
    "s_add_u32 s86, s86, 128\n\t"
    "s_cmp_eq_u32 s89, 7\n\t"
    "s_cselect_b32 s89, 0, s89\n\t"
    "s_cselect_b32 s86, 0, s86\n\t"
    "ds_read_b128 v[16:19], v15\n\t"
    "s_waitcnt lgkmcnt(7)\n\t"
    "v_mfma_f32_32x32x16_bf16 v[120:135], v[36:39], v[76:79], v[120:135]\n\t"
    "ds_read_b128 v[20:23], v15 offset:32\n\t"
    "s_waitcnt lgkmcnt(7)\n\t"
    "v_mfma_f32_32x32x16_bf16 v[120:135], v[40:43], v[80:83], v[120:135]\n\t"
    "ds_read_b128 v[24:27], v15 offset:64\n\t"
    "s_waitcnt lgkmcnt(7)\n\t"
    "v_mfma_f32_32x32x16_bf16 v[120:135], v[44:47], v[84:87], v[120:135]\n\t"
    "ds_read_b128 v[28:31], v15 offset:96\n\t"
    "s_waitcnt lgkmcnt(7)\n\t"
    "v_mfma_f32_32x32x16_bf16 v[120:135], v[48:51], v[88:91], v[120:135]\n\t"
    "s_waitcnt lgkmcnt(6)\n\t"
    "v_mfma_f32_32x32x16_bf16 v[120:135], v[52:55], v[92:95], v[120:135]\n\t"
    "s_waitcnt lgkmcnt(5)\n\t"
    "v_mfma_f32_32x32x16_bf16 v[120:135], v[56:59], v[96:99], v[120:135]\n\t"
    "s_waitcnt lgkmcnt(4)\n\t"
    "v_mfma_f32_32x32x16_bf16 v[120:135], v[60:63], v[100:103], v[120:135]\n\t"
    "s_waitcnt lgkmcnt(3)\n\t"
    "v_mfma_f32_32x32x16_bf16 v[120:135], v[16:19], v[104:107], v[120:135]\n\t"
    "s_waitcnt lgkmcnt(2)\n\t"
    "v_mfma_f32_32x32x16_bf16 v[120:135], v[20:23], v[108:111], v[120:135]\n\t"
    "s_waitcnt lgkmcnt(1)\n\t"
    "v_mfma_f32_32x32x16_bf16 v[120:135], v[24:27], v[112:115], v[120:135]\n\t"
    "s_waitcnt lgkmcnt(0)\n\t"
    "v_mfma_f32_32x32x16_bf16 v[120:135], v[28:31], v[116:119], v[120:135]\n\t"
    "s_nop 11\n\t"
    "v_mul_f32 v165, 0xbfb8aa3b, v120\n\t"
    "v_exp_f32 v165, v165\n\t"
    "s_nop 0\n\t"
    "v_add_f32 v165, 1.0, v165\n\t"
    "v_rcp_f32 v165, v165\n\t"
    "v_lshrrev_b32 v164, 2, v252\n\t"
    "s_mov_b32 exec_hi, 0\n\t"
    "s_nop 0\n\t"
    "buffer_store_dword v165, v164, %[rw], s62 offen sc1\n\t"
    "s_mov_b64 exec, -1\n\t"
    "s_branch Lqdone_%=\n\t"
    "Lduty_%=:\n\t"
    "s_setprio 2\n\t"
    "s_waitcnt lgkmcnt(6)\n\t"
    "v_mfma_f32_32x32x16_bf16 v[48:63], v[168:171], v[136:139], 0\n\t"
    "v_add_u32 v221, 8192, v221\n\t"
    "ds_read_b64_tr_b16 v[152:153], v221\n\t"
    "ds_read_b64_tr_b16 v[154:155], v221 offset:512\n\t"
    "s_waitcnt lgkmcnt(6)\n\t"
    "v_mfma_f32_32x32x16_bf16 v[48:63], v[172:175], v[140:143], v[48:63]\n\t"
    "ds_read_b64_tr_b16 v[156:157], v221 offset:2048\n\t"
    "ds_read_b64_tr_b16 v[158:159], v221 offset:2560\n\t"
    "s_waitcnt lgkmcnt(6)\n\t"
    "v_mfma_f32_32x32x16_bf16 v[48:63], v[224:227], v[144:147], v[48:63]\n\t"
    "ds_read_b64_tr_b16 v[136:137], v221 offset:4096\n\t"
    "ds_read_b64_tr_b16 v[138:139], v221 offset:4608\n\t"
    "s_waitcnt lgkmcnt(6)\n\t"
    "v_mfma_f32_32x32x16_bf16 v[48:63], v[228:231], v[148:151], v[48:63]\n\t"
    "ds_read_b64_tr_b16 v[140:141], v221 offset:6144\n\t"
    "ds_read_b64_tr_b16 v[142:143], v221 offset:6656\n\t"
    "s_add_u32 s81, s72, 128\n\t"
    "v_add_u32 v236, s81, v242\n\t"
    "v_mul_u32_u24 v237, 2341, v236\n\t"
    "v_lshrrev_b32 v237, 16, v237\n\t"
    "v_mul_u32_u24 v238, 28, v237\n\t"
    "v_sub_u32 v236, v236, v238\n\t"
    "s_waitcnt lgkmcnt(6)\n\t"
    "v_mfma_f32_32x32x16_bf16 v[48:63], v[184:187], v[152:155], v[48:63]\n\t"
    "v_add_u32 v221, 8192, v221\n\t"
    "ds_read_b64_tr_b16 v[144:145], v221\n\t"
    "ds_read_b64_tr_b16 v[146:147], v221 offset:512\n\t"
    "v_lshl_add_u32 v237, v237, 18, v243\n\t"
    "v_lshl_add_u32 v236, v236, 4, v237\n\t"
    "v_cvt_pk_bf16_f32 v160, v32, v33\n\t"
    "v_cvt_pk_bf16_f32 v161, v34, v35\n\t"
    "v_cvt_pk_bf16_f32 v162, v36, v37\n\t"
    "s_waitcnt lgkmcnt(6)\n\t"
    "v_mfma_f32_32x32x16_bf16 v[48:63], v[188:191], v[156:159], v[48:63]\n\t"
    "ds_read_b64_tr_b16 v[148:149], v221 offset:2048\n\t"
    "ds_read_b64_tr_b16 v[150:151], v221 offset:2560\n\t"
    "v_cvt_pk_bf16_f32 v163, v38, v39\n\t"
    "buffer_store_dwordx4 v[160:163], v236, %[rw], 0 offen sc1\n\t"
    "v_cvt_pk_bf16_f32 v164, v40, v41\n\t"
    "s_waitcnt lgkmcnt(6)\n\t"
    "v_mfma_f32_32x32x16_bf16 v[48:63], v[176:179], v[136:139], v[48:63]\n\t"
    "ds_read_b64_tr_b16 v[152:153], v221 offset:4096\n\t"
    "ds_read_b64_tr_b16 v[154:155], v221 offset:4608\n\t"
    "v_cvt_pk_bf16_f32 v165, v42, v43\n\t"
    "v_cvt_pk_bf16_f32 v166, v44, v45\n\t"
    "v_cvt_pk_bf16_f32 v167, v46, v47\n\t"
    "buffer_store_dwordx4 v[164:167], v236, %[rw], 0 offen offset:1024 sc1\n\t"
    "s_waitcnt lgkmcnt(6)\n\t"
    "v_mfma_f32_32x32x16_bf16 v[48:63], v[180:183], v[140:143], v[48:63]\n\t"
    "ds_read_b64_tr_b16 v[156:157], v221 offset:6144\n\t"
    "ds_read_b64_tr_b16 v[158:159], v221 offset:6656\n\t"
    "s_waitcnt lgkmcnt(6)\n\t"
    "v_mfma_f32_32x32x16_bf16 v[48:63], v[192:195], v[144:147], v[48:63]\n\t"
    "v_add_u32 v221, 8192, v221\n\t"
    "ds_read_b64_tr_b16 v[136:137], v221\n\t"
    "ds_read_b64_tr_b16 v[138:139], v221 offset:512\n\t"
    "s_waitcnt lgkmcnt(6)\n\t"
    "v_mfma_f32_32x32x16_bf16 v[48:63], v[196:199], v[148:151], v[48:63]\n\t"
    "ds_read_b64_tr_b16 v[140:141], v221 offset:2048\n\t"
    "ds_read_b64_tr_b16 v[142:143], v221 offset:2560\n\t"
    "s_waitcnt lgkmcnt(6)\n\t"
    "v_mfma_f32_32x32x16_bf16 v[48:63], v[200:203], v[152:155], v[48:63]\n\t"
    "ds_read_b64_tr_b16 v[144:145], v221 offset:4096\n\t"
    "ds_read_b64_tr_b16 v[146:147], v221 offset:4608\n\t"
    "s_waitcnt lgkmcnt(6)\n\t"
    "v_mfma_f32_32x32x16_bf16 v[48:63], v[204:207], v[156:159], v[48:63]\n\t"
    "ds_read_b64_tr_b16 v[148:149], v221 offset:6144\n\t"
    "ds_read_b64_tr_b16 v[150:151], v221 offset:6656\n\t"
    "s_waitcnt lgkmcnt(6)\n\t"
    "v_mfma_f32_32x32x16_bf16 v[48:63], v[208:211], v[136:139], v[48:63]\n\t"
    "v_add_u32 v221, 8192, v221\n\t"
    "ds_read_b64_tr_b16 v[152:153], v221\n\t"
    "ds_read_b64_tr_b16 v[154:155], v221 offset:512\n\t"
    "s_waitcnt lgkmcnt(6)\n\t"
    "v_mfma_f32_32x32x16_bf16 v[48:63], v[212:215], v[140:143], v[48:63]\n\t"
    "ds_read_b64_tr_b16 v[156:157], v221 offset:2048\n\t"
    "ds_read_b64_tr_b16 v[158:159], v221 offset:2560\n\t"
    "s_waitcnt lgkmcnt(6)\n\t"
    "v_mfma_f32_32x32x16_bf16 v[48:63], v[64:67], v[144:147], v[48:63]\n\t"
    "ds_read_b64_tr_b16 v[136:137], v221 offset:4096\n\t"
    "ds_read_b64_tr_b16 v[138:139], v221 offset:4608\n\t"
    "s_waitcnt lgkmcnt(6)\n\t"
    "v_mfma_f32_32x32x16_bf16 v[48:63], v[68:71], v[148:151], v[48:63]\n\t"
    "ds_read_b64_tr_b16 v[140:141], v221 offset:6144\n\t"
    "ds_read_b64_tr_b16 v[142:143], v221 offset:6656\n\t"
    "s_waitcnt lgkmcnt(6)\n\t"
    "v_mfma_f32_32x32x16_bf16 v[48:63], v[72:75], v[152:155], v[48:63]\n\t"
    "v_add_u32 v221, -155648, v221\n\t"
    "ds_read_b64_tr_b16 v[144:145], v221\n\t"
    "ds_read_b64_tr_b16 v[146:147], v221 offset:512\n\t"
    "s_waitcnt lgkmcnt(6)\n\t"
    "v_mfma_f32_32x32x16_bf16 v[48:63], v[76:79], v[156:159], v[48:63]\n\t"
    "ds_read_b64_tr_b16 v[148:149], v221 offset:2048\n\t"
    "ds_read_b64_tr_b16 v[150:151], v221 offset:2560\n\t"
    "s_waitcnt lgkmcnt(6)\n\t"
    "v_mfma_f32_32x32x16_bf16 v[48:63], v[80:83], v[136:139], v[48:63]\n\t"
    "ds_read_b64_tr_b16 v[152:153], v221 offset:4096\n\t"
    "ds_read_b64_tr_b16 v[154:155], v221 offset:4608\n\t"
    "s_waitcnt lgkmcnt(6)\n\t"
    "v_mfma_f32_32x32x16_bf16 v[48:63], v[84:87], v[140:143], v[48:63]\n\t"
    "ds_read_b64_tr_b16 v[156:157], v221 offset:6144\n\t"
    "ds_read_b64_tr_b16 v[158:159], v221 offset:6656\n\t"
    "s_waitcnt lgkmcnt(6)\n\t"
    "v_mfma_f32_32x32x16_bf16 v[48:63], v[88:91], v[144:147], v[48:63]\n\t"
    "v_add_u32 v221, 8192, v221\n\t"
    "ds_read_b64_tr_b16 v[136:137], v221\n\t"
    "ds_read_b64_tr_b16 v[138:139], v221 offset:512\n\t"
    "s_waitcnt lgkmcnt(6)\n\t"
    "v_mfma_f32_32x32x16_bf16 v[48:63], v[92:95], v[148:151], v[48:63]\n\t"
    "ds_read_b64_tr_b16 v[140:141], v221 offset:2048\n\t"
    "ds_read_b64_tr_b16 v[142:143], v221 offset:2560\n\t"
    "s_waitcnt lgkmcnt(6)\n\t"
    "v_mfma_f32_32x32x16_bf16 v[48:63], v[96:99], v[152:155], v[48:63]\n\t"
    "ds_read_b64_tr_b16 v[144:145], v221 offset:4096\n\t"
    "ds_read_b64_tr_b16 v[146:147], v221 offset:4608\n\t"
    "s_waitcnt lgkmcnt(6)\n\t"
    "v_mfma_f32_32x32x16_bf16 v[48:63], v[100:103], v[156:159], v[48:63]\n\t"
    "ds_read_b64_tr_b16 v[148:149], v221 offset:6144\n\t"
    "ds_read_b64_tr_b16 v[150:151], v221 offset:6656\n\t"
    "s_waitcnt lgkmcnt(6)\n\t"
    "v_mfma_f32_32x32x16_bf16 v[48:63], v[104:107], v[136:139], v[48:63]\n\t"
    "s_waitcnt lgkmcnt(4)\n\t"
    "v_mfma_f32_32x32x16_bf16 v[48:63], v[108:111], v[140:143], v[48:63]\n\t"
    "s_waitcnt lgkmcnt(2)\n\t"
    "v_mfma_f32_32x32x16_bf16 v[48:63], v[112:115], v[144:147], v[48:63]\n\t"
    "s_waitcnt lgkmcnt(0)\n\t"
    "v_mfma_f32_32x32x16_bf16 v[48:63], v[116:119], v[148:151], v[48:63]\n\t"
    "s_and_b32 s90, %[wv], 4\n\t"
    "s_lshl_b32 s90, s90, 3\n\t"
    "s_add_u32 s81, s90, 192\n\t"
    "v_add_u32 v236, s81, v242\n\t"
    "v_mul_u32_u24 v237, 2341, v236\n\t"
    "v_lshrrev_b32 v237, 16, v237\n\t"
    "v_mul_u32_u24 v238, 28, v237\n\t"
    "v_sub_u32 v236, v236, v238\n\t"
    "v_lshl_add_u32 v237, v237, 18, v243\n\t"
    "v_lshl_add_u32 v236, v236, 4, v237\n\t"
    "s_nop 1\n\t"
    "v_cvt_pk_bf16_f32 v160, v48, v49\n\t"
    "v_cvt_pk_bf16_f32 v161, v50, v51\n\t"
    "v_cvt_pk_bf16_f32 v162, v52, v53\n\t"
    "v_cvt_pk_bf16_f32 v163, v54, v55\n\t"
    "buffer_store_dwordx4 v[160:163], v236, %[rw], 0 offen sc1\n\t"
    "v_cvt_pk_bf16_f32 v164, v56, v57\n\t"
    "v_cvt_pk_bf16_f32 v165, v58, v59\n\t"
    "v_cvt_pk_bf16_f32 v166, v60, v61\n\t"
    "v_cvt_pk_bf16_f32 v167, v62, v63\n\t"
    "buffer_store_dwordx4 v[164:167], v236, %[rw], 0 offen offset:1024 sc1\n\t"
    "s_setprio 0\n\t"
    "Lqdone_%=:\n\t"
    "buffer_load_dwordx2 v[0:1], v245, %[rw], 0 offen sc1\n\t"
    "s_waitcnt vmcnt(1)\n\t"
    "s_waitcnt lgkmcnt(0)\n\t"
    "s_barrier\n\t"
    "s_cmp_eq_u32 %[wv], 7\n\t"
    "s_cbranch_scc0 Lpubpq_%=\n\t"
    "s_mov_b64 exec, 1\n\t"
    "s_add_u32 s80, s71, 32768\n\t"
    "v_mov_b32 v160, s84\n\t"
    "v_mov_b32 v161, s85\n\t"
    "buffer_store_dwordx2 v[160:161], off, %[rw], s80 sc1\n\t"
    "s_mov_b64 exec, -1\n\t"
    "Lpubpq_%=:\n\t"
    "s_cmp_eq_u32 %[wv], 6\n\t"
    "s_cbranch_scc0 Lpubpqa_%=\n\t"
    "s_mov_b64 exec, 1\n\t"
    "s_add_u32 s80, s71, 0\n\t"
    "v_mov_b32 v162, s84\n\t"
    "v_mov_b32 v163, s85\n\t"
    "buffer_store_dwordx2 v[162:163], off, %[rw], s80 sc1\n\t"
    "s_mov_b64 exec, -1\n\t"
    "Lpubpqa_%=:\n\t"
    "v_mov_b32 v12, 131072\n\t"
    "v_mov_b32 v13, 0\n\t"
    "ds_write_b32 v12, v13\n\t"
    "s_waitcnt vmcnt(0)\n\t"
    "s_mov_b32 s81, 0\n\t"
    "Lpb_%=:\n\t"
    "v_cmp_eq_u64 vcc, s[84:85], v[0:1]\n\t"
    "s_cmp_eq_u32 vcc_lo, -1\n\t"
    "s_cbranch_scc1 Lgb_%=\n\t"
    "buffer_load_dwordx2 v[0:1], v245, %[rw], 0 offen sc1\n\t"
    "s_add_u32 s81, s81, 1\n\t"
    "s_waitcnt vmcnt(0)\n\t"
    "s_cmp_lt_u32 s81, 0x60000\n\t"
    "s_cbranch_scc1 Lpb_%=\n\t"
    "Lgb_%=:\n\t"
    "buffer_load_dwordx4 v[168:171], v252, %[rw], s56 offen sc1\n\t"
    "buffer_load_dwordx4 v[172:175], v252, %[rw], s56 offen offset:1024 sc1\n\t"
    "buffer_load_dwordx4 v[224:227], v252, %[rw], s61 offen sc1\n\t"
    "buffer_load_dwordx4 v[228:231], v252, %[rw], s61 offen offset:1024 sc1\n\t"
    "buffer_load_dwordx4 v[184:187], v252, %[rw], s50 offen sc1\n\t"
    "buffer_load_dwordx4 v[188:191], v252, %[rw], s50 offen offset:1024 sc1\n\t"
    "s_add_u32 m0, s70, 16384\n\t"
    "s_add_u32 s80, s59, 0\n\t"
    "buffer_load_dwordx4 v248, %[rd], s80 offen lds\n\t"
    "s_add_u32 s80, s59, 6144\n\t"
    "buffer_load_dwordx4 v248, %[rd], s80 offen offset:1024 lds\n\t"
    "s_add_u32 m0, s70, 32768\n\t"
    "s_add_u32 s80, s59, 229376\n\t"
    "buffer_load_dwordx4 v248, %[rd], s80 offen lds\n\t"
    "s_add_u32 s80, s59, 235520\n\t"
    "buffer_load_dwordx4 v248, %[rd], s80 offen offset:1024 lds\n\t"
    "s_add_u32 m0, s70, 49152\n\t"
    "s_add_u32 s80, s59, 458752\n\t"
    "buffer_load_dwordx4 v248, %[rd], s80 offen lds\n\t"
    "s_add_u32 s80, s59, 464896\n\t"
    "buffer_load_dwordx4 v248, %[rd], s80 offen offset:1024 lds\n\t"
    "s_cmp_lt_u32 %[wv], 4\n\t"
    "s_cbranch_scc0 Lnorefb_%=\n\t"
    "s_waitcnt vmcnt(8)\n\t"
    "s_bitcmp1_b32 %[ty], 4\n\t"
    "s_cselect_b32 s94, -1, 0\n\t"
    "s_mov_b32 s95, 0\n\t"
    "s_mov_b32 s96, 0\n\t"
    "s_cselect_b32 s97, 0, -1\n\t"
    "v_cndmask_b32 v224, v224, 0, s[94:95]\n\t"
    "v_cndmask_b32 v225, v225, 0, s[94:95]\n\t"
    "v_cndmask_b32 v230, v230, 0, s[96:97]\n\t"
    "v_cndmask_b32 v231, v231, 0, s[96:97]\n\t"
    "s_lshl_b32 s80, %[wv], 11\n\t"
    "v_add_u32 v242, s80, v246\n\t"
    "v_mfma_f32_32x32x16_bf16 v[16:31], v[168:171], v[224:227], 0\n\t"
    "v_mfma_f32_32x32x16_bf16 v[16:31], v[172:175], v[228:231], v[16:31]\n\t"
    "s_nop 11\n\t"
    "v_max3_f32 v32, v16, v17, v18\n\t"
    "v_max3_f32 v33, v19, v20, v21\n\t"
    "v_max3_f32 v32, v32, v22, v23\n\t"
    "v_max3_f32 v33, v33, v24, v25\n\t"
    "v_max3_f32 v32, v32, v26, v27\n\t"
    "v_max3_f32 v33, v33, v28, v29\n\t"
    "v_max3_f32 v32, v32, v30, v31\n\t"
    "v_max_f32 v32, v32, v33\n\t"
    "v_mov_b32 v33, v32\n\t"
    "s_nop 1\n\t"
    "v_permlane32_swap_b32 v32, v33\n\t"
    "s_nop 1\n\t"
    "v_min_f32 v32, -v32, -v33\n\t"
    "v_and_b32 v33, 0xffff0000, v32\n\t"
    "v_sub_f32 v33, v32, v33\n\t"
    "v_and_b32 v34, 0xffff0000, v33\n\t"
    "v_lshrrev_b32 v32, 16, v32\n\t"
    "v_sub_f32 v33, v33, v34\n\t"
    "v_or_b32 v32, v32, v34\n\t"
    "v_lshrrev_b32 v33, 16, v33\n\t"
    "v_cndmask_b32 v224, v224, v32, s[94:95]\n\t"
    "v_cndmask_b32 v225, v225, v33, s[94:95]\n\t"
    "v_cndmask_b32 v230, v230, v32, s[96:97]\n\t"
    "v_cndmask_b32 v231, v231, v33, s[96:97]\n\t"
    "ds_write_b128 v242, v[224:227]\n\t"
    "ds_write_b128 v242, v[228:231] offset:1024\n\t"
    "Lnorefb_%=:\n\t"
    "Ljoin_%=:\n\t"
    "s_mov_b32 s94, 0\n\t"
    "s_mov_b32 s95, -1\n\t"
    "v_add_u32 v32, 0x8000, v251\n\t"
    "v_cndmask_b32 v32, v32, v245, s[94:95]\n\t"
    "s_nop 6\n\t"
    "v_mul_f32 v48, v48, v120\n\t"
    "v_mul_f32 v49, v49, v121\n\t"
    "v_mul_f32 v50, v50, v122\n\t"
    "v_mul_f32 v51, v51, v123\n\t"
    "v_mul_f32 v52, v52, v124\n\t"
    "v_mul_f32 v53, v53, v125\n\t"
    "v_mul_f32 v54, v54, v126\n\t"
    "v_mul_f32 v55, v55, v127\n\t"
    "v_cvt_pk_bf16_f32 v24, v48, v49\n\t"
    "v_cvt_pk_bf16_f32 v25, v50, v51\n\t"
    "v_cvt_pk_bf16_f32 v26, v52, v53\n\t"
    "v_cvt_pk_bf16_f32 v27, v54, v55\n\t"
    "v_mul_f32 v56, v56, v128\n\t"
    "v_mul_f32 v57, v57, v129\n\t"
    "v_mul_f32 v58, v58, v130\n\t"
    "v_mul_f32 v59, v59, v131\n\t"
    "v_mul_f32 v60, v60, v132\n\t"
    "v_mul_f32 v61, v61, v133\n\t"
    "v_mul_f32 v62, v62, v134\n\t"
    "v_mul_f32 v63, v63, v135\n\t"
    "v_cvt_pk_bf16_f32 v28, v56, v57\n\t"
    "v_cvt_pk_bf16_f32 v29, v58, v59\n\t"
    "v_cvt_pk_bf16_f32 v30, v60, v61\n\t"
    "v_cvt_pk_bf16_f32 v31, v62, v63\n\t"
    "s_add_u32 s46, s56, 20480\n\t"
    "s_add_u32 s47, s56, 24576\n\t"
    "s_add_u32 s48, s56, 28672\n\t"
    "buffer_load_dwordx2 v[254:255], v32, %[rw], 0 offen sc1\n\t"
    "s_waitcnt vmcnt(9)\n\t"
    "s_waitcnt lgkmcnt(0)\n\t"
    "s_barrier\n\t"
    "ds_read_b128 v[224:227], v246\n\t"
    "ds_read_b128 v[228:231], v246 offset:1024\n\t"
    "ds_read_b128 v[232:235], v246 offset:2048\n\t"
    "ds_read_b128 v[236:239], v246 offset:3072\n\t"
    "ds_read_b128 v[160:163], v246 offset:6144\n\t"
    "ds_read_b128 v[164:167], v246 offset:7168\n\t"
    "ds_read_b128 v[240:243], v246 offset:4096\n\t"
    "ds_read_b128 v[244:247], v246 offset:5120\n\t"
    "s_waitcnt lgkmcnt(7)\n\t"
    "v_mfma_f32_32x32x16_bf16 v[128:143], v[168:171], v[224:227], 0\n\t"
    "s_waitcnt lgkmcnt(6)\n\t"
    "v_mfma_f32_32x32x16_bf16 v[128:143], v[172:175], v[228:231], v[128:143]\n\t"
    "s_cmp_lt_u32 %[wv], 4\n\t"
    "s_cbranch_scc1 Lpr0_%=\n\t"
    "s_setprio 1\n\t"
    "Lpr0_%=:\n\t"
    "s_waitcnt lgkmcnt(5)\n\t"
    "v_mfma_f32_32x32x16_bf16 v[144:159], v[168:171], v[232:235], 0\n\t"
    "s_nop 7\n\t"
    "v_exp_f32 v128, v128\n\t"
    "v_exp_f32 v129, v129\n\t"
    "v_exp_f32 v130, v130\n\t"
    "v_exp_f32 v131, v131\n\t"
    "v_and_b32 v37, 0x300, v252\n\t"
    "v_and_b32 v36, 0xf0, v252\n\t"
    "v_lshl_or_b32 v36, v37, 1, v36\n\t"
    "v_mov_b32 v216, -1\n\t"
    "v_mov_b32 v217, -1\n\t"
    "v_mov_b32 v218, -1\n\t"
    "v_mov_b32 v219, -1\n\t"
    "buffer_store_dwordx4 v[216:219], v36, %[rw], s66 offen sc1\n\t"
    "v_exp_f32 v132, v132\n\t"
    "v_exp_f32 v133, v133\n\t"
    "v_exp_f32 v134, v134\n\t"
    "v_exp_f32 v135, v135\n\t"
    "v_cvt_pk_bf16_f32 v0, v128, v129\n\t"
    "v_cvt_pk_bf16_f32 v1, v130, v131\n\t"
    "s_waitcnt lgkmcnt(4)\n\t"
    "v_mfma_f32_32x32x16_bf16 v[144:159], v[172:175], v[236:239], v[144:159]\n\t"
    "v_exp_f32 v136, v136\n\t"
    "v_exp_f32 v137, v137\n\t"
    "v_exp_f32 v138, v138\n\t"
    "v_exp_f32 v139, v139\n\t"
    "v_cvt_pk_bf16_f32 v2, v132, v133\n\t"
    "v_cvt_pk_bf16_f32 v3, v134, v135\n\t"
    "v_exp_f32 v140, v140\n\t"
    "v_exp_f32 v141, v141\n\t"
    "v_exp_f32 v142, v142\n\t"
    "v_exp_f32 v143, v143\n\t"
    "v_cvt_pk_bf16_f32 v4, v136, v137\n\t"
    "v_cvt_pk_bf16_f32 v5, v138, v139\n\t"
    "v_cvt_pk_bf16_f32 v6, v140, v141\n\t"
    "v_cvt_pk_bf16_f32 v7, v142, v143\n\t"
    "s_waitcnt vmcnt(1)\n\t"
    "s_mov_b32 s81, 0\n\t"
    "Lpb0_%=:\n\t"
    "v_cmp_eq_u64 vcc, s[84:85], v[254:255]\n\t"
    "s_or_b32 vcc_lo, vcc_lo, 0xffffff80\n\t"
    "s_cmp_eq_u64 vcc, -1\n\t"
    "s_cbranch_scc1 Lgb0_%=\n\t"
    "buffer_load_dwordx2 v[254:255], v32, %[rw], 0 offen sc1\n\t"
    "s_add_u32 s81, s81, 1\n\t"
    "s_waitcnt vmcnt(0)\n\t"
    "s_cmp_lt_u32 s81, 0x60000\n\t"
    "s_cbranch_scc1 Lpb0_%=\n\t"
    "Lgb0_%=:\n\t"
    "s_waitcnt lgkmcnt(1)\n\t"
    "v_mfma_f32_32x32x16_bf16 v[128:143], v[168:171], v[240:243], 0\n\t"
    "buffer_load_dwordx4 v[176:179], v252, %[rw], s56 offen offset:2048 sc1\n\t"
    "v_exp_f32 v144, v144\n\t"
    "v_exp_f32 v145, v145\n\t"
    "v_exp_f32 v146, v146\n\t"
    "v_exp_f32 v147, v147\n\t"
    "buffer_load_dwordx4 v[180:183], v252, %[rw], s56 offen offset:3072 sc1\n\t"
    "v_exp_f32 v148, v148\n\t"
    "v_exp_f32 v149, v149\n\t"
    "v_exp_f32 v150, v150\n\t"
    "v_exp_f32 v151, v151\n\t"
    "v_cvt_pk_bf16_f32 v8, v144, v145\n\t"
    "v_cvt_pk_bf16_f32 v9, v146, v147\n\t"
    "s_waitcnt lgkmcnt(0)\n\t"
    "v_mfma_f32_32x32x16_bf16 v[128:143], v[172:175], v[244:247], v[128:143]\n\t"
    "buffer_load_dwordx4 v[200:203], v252, %[rw], s57 offen sc1\n\t"
    "v_exp_f32 v152, v152\n\t"
    "v_exp_f32 v153, v153\n\t"
    "v_exp_f32 v154, v154\n\t"
    "v_exp_f32 v155, v155\n\t"
    "v_cvt_pk_bf16_f32 v10, v148, v149\n\t"
    "v_cvt_pk_bf16_f32 v11, v150, v151\n\t"
    "buffer_load_dwordx4 v[204:207], v252, %[rw], s57 offen offset:1024 sc1\n\t"
    "v_exp_f32 v156, v156\n\t"
    "v_exp_f32 v157, v157\n\t"
    "v_exp_f32 v158, v158\n\t"
    "v_exp_f32 v159, v159\n\t"
    "v_cvt_pk_bf16_f32 v12, v152, v153\n\t"
    "v_cvt_pk_bf16_f32 v13, v154, v155\n\t"
    "v_cvt_pk_bf16_f32 v14, v156, v157\n\t"
    "v_cvt_pk_bf16_f32 v15, v158, v159\n\t"
    "v_mfma_f32_32x32x16_bf16 v[144:159], v[168:171], v[160:163], 0\n\t"
    "buffer_load_dwordx4 v[192:195], v252, %[rw], s50 offen offset:2048 sc1\n\t"
    "v_exp_f32 v128, v128\n\t"
    "v_exp_f32 v129, v129\n\t"
    "v_exp_f32 v130, v130\n\t"
    "v_exp_f32 v131, v131\n\t"
    "buffer_load_dwordx4 v[196:199], v252, %[rw], s50 offen offset:3072 sc1\n\t"
    "v_exp_f32 v132, v132\n\t"
    "v_exp_f32 v133, v133\n\t"
    "v_exp_f32 v134, v134\n\t"
    "v_exp_f32 v135, v135\n\t"
    "v_cvt_pk_bf16_f32 v16, v128, v129\n\t"
    "v_cvt_pk_bf16_f32 v17, v130, v131\n\t"
    "v_mfma_f32_32x32x16_bf16 v[144:159], v[172:175], v[164:167], v[144:159]\n\t"
    "buffer_load_dwordx4 v[208:211], v252, %[rw], s57 offen offset:2048 sc1\n\t"
    "v_exp_f32 v136, v136\n\t"
    "v_exp_f32 v137, v137\n\t"
    "v_exp_f32 v138, v138\n\t"
    "v_exp_f32 v139, v139\n\t"
    "v_cvt_pk_bf16_f32 v18, v132, v133\n\t"
    "v_cvt_pk_bf16_f32 v19, v134, v135\n\t"
    "buffer_load_dwordx4 v[168:171], v252, %[rw], s51 offen sc1\n\t"
    "v_exp_f32 v140, v140\n\t"
    "v_exp_f32 v141, v141\n\t"
    "v_exp_f32 v142, v142\n\t"
    "v_exp_f32 v143, v143\n\t"
    "v_cvt_pk_bf16_f32 v20, v136, v137\n\t"
    "v_cvt_pk_bf16_f32 v21, v138, v139\n\t"
    "v_cvt_pk_bf16_f32 v22, v140, v141\n\t"
    "v_cvt_pk_bf16_f32 v23, v142, v143\n\t"
    "v_mfma_f32_32x32x16_bf16 v[128:143], v[184:187], v[224:227], 0\n\t"
    "buffer_load_dwordx4 v[212:215], v252, %[rw], s57 offen offset:3072 sc1\n\t"
    "v_exp_f32 v144, v144\n\t"
    "v_exp_f32 v145, v145\n\t"
    "v_exp_f32 v146, v146\n\t"
    "v_mfma_f32_32x32x16_bf16 v[128:143], v[188:191], v[228:231], v[128:143]\n\t"
    "buffer_load_dwordx4 v[172:175], v252, %[rw], s51 offen offset:1024 sc1\n\t"
    "v_exp_f32 v147, v147\n\t"
    "v_exp_f32 v148, v148\n\t"
    "v_cvt_pk_bf16_f32 v144, v144, v145\n\t"
    "s_waitcnt vmcnt(9)\n\t"
    "v_mfma_f32_32x32x16_bf16 v[64:79], v[176:179], v[0:3], 0\n\t"
    "v_exp_f32 v149, v149\n\t"
    "v_exp_f32 v150, v150\n\t"
    "v_cvt_pk_bf16_f32 v145, v146, v147\n\t"
    "v_mfma_f32_32x32x16_bf16 v[80:95], v[176:179], v[8:11], 0\n\t"
    "s_add_u32 s80, s65, 14336\n\t"
    "buffer_store_dwordx4 v[24:27], v252, %[rw], s80 offen sc1\n\t"
    "v_exp_f32 v151, v151\n\t"
    "v_exp_f32 v152, v152\n\t"
    "v_cvt_pk_bf16_f32 v146, v148, v149\n\t"
    "v_mfma_f32_32x32x16_bf16 v[96:111], v[176:179], v[16:19], 0\n\t"
    "v_exp_f32 v153, v153\n\t"
    "v_exp_f32 v154, v154\n\t"
    "v_cvt_pk_bf16_f32 v147, v150, v151\n\t"
    "s_waitcnt vmcnt(9)\n\t"
    "v_mfma_f32_32x32x16_bf16 v[64:79], v[180:183], v[4:7], v[64:79]\n\t"
    "v_exp_f32 v155, v155\n\t"
    "v_exp_f32 v156, v156\n\t"
    "v_cvt_pk_bf16_f32 v148, v152, v153\n\t"
    "v_mfma_f32_32x32x16_bf16 v[80:95], v[180:183], v[12:15], v[80:95]\n\t"
    "v_exp_f32 v157, v157\n\t"
    "v_exp_f32 v158, v158\n\t"
    "v_cvt_pk_bf16_f32 v149, v154, v155\n\t"
    "v_mfma_f32_32x32x16_bf16 v[96:111], v[180:183], v[20:23], v[96:111]\n\t"
    "v_exp_f32 v159, v159\n\t"
    "v_cvt_pk_bf16_f32 v150, v156, v157\n\t"
    "v_cvt_pk_bf16_f32 v151, v158, v159\n\t"
    "v_mfma_f32_32x32x16_bf16 v[112:127], v[176:179], v[144:147], 0\n\t"
    "v_exp_f32 v128, v128\n\t"
    "v_exp_f32 v129, v129\n\t"
    "v_exp_f32 v130, v130\n\t"
    "v_mfma_f32_32x32x16_bf16 v[112:127], v[180:183], v[148:151], v[112:127]\n\t"
    "v_exp_f32 v131, v131\n\t"
    "v_exp_f32 v132, v132\n\t"
    "v_exp_f32 v133, v133\n\t"
    "v_cvt_pk_bf16_f32 v128, v128, v129\n\t"
    "v_mfma_f32_32x32x16_bf16 v[144:159], v[184:187], v[232:235], 0\n\t"
    "s_add_u32 s80, s58, 14336\n\t"
    "buffer_store_dwordx4 v[28:31], v252, %[rw], s80 offen sc1\n\t"
    "v_exp_f32 v134, v134\n\t"
    "v_exp_f32 v135, v135\n\t"
    "v_exp_f32 v136, v136\n\t"
    "v_cvt_pk_bf16_f32 v129, v130, v131\n\t"
    "v_cvt_pk_bf16_f32 v130, v132, v133\n\t"
    "v_mfma_f32_32x32x16_bf16 v[144:159], v[188:191], v[236:239], v[144:159]\n\t"
    "buffer_load_dwordx4 v[176:179], v252, %[rw], s51 offen offset:2048 sc1\n\t"
    "v_exp_f32 v137, v137\n\t"
    "v_exp_f32 v138, v138\n\t"
    "v_exp_f32 v139, v139\n\t"
    "v_exp_f32 v140, v140\n\t"
    "v_exp_f32 v141, v141\n\t"
    "v_exp_f32 v142, v142\n\t"
    "v_exp_f32 v143, v143\n\t"
    "v_cvt_pk_bf16_f32 v131, v134, v135\n\t"
    "v_cvt_pk_bf16_f32 v132, v136, v137\n\t"
    "v_cvt_pk_bf16_f32 v133, v138, v139\n\t"
    "v_cvt_pk_bf16_f32 v134, v140, v141\n\t"
    "v_cvt_pk_bf16_f32 v135, v142, v143\n\t"
    "s_cmp_lt_u32 %[wv], 4\n\t"
    "s_cbranch_scc1 Lpr5_%=\n\t"
    "s_setprio 0\n\t"
    "Lpr5_%=:\n\t"
    "s_waitcnt vmcnt(8)\n\t"
    "v_mfma_f32_32x32x16_bf16 v[64:79], v[192:195], v[128:131], v[64:79]\n\t"
    "v_exp_f32 v144, v144\n\t"
    "v_exp_f32 v145, v145\n\t"
    "v_exp_f32 v146, v146\n\t"
    "s_waitcnt vmcnt(7)\n\t"
    "v_mfma_f32_32x32x16_bf16 v[64:79], v[196:199], v[132:135], v[64:79]\n\t"
    "buffer_load_dwordx4 v[180:183], v252, %[rw], s51 offen offset:3072 sc1\n\t"
    "v_exp_f32 v147, v147\n\t"
    "v_exp_f32 v148, v148\n\t"
    "v_exp_f32 v149, v149\n\t"
    "v_cvt_pk_bf16_f32 v144, v144, v145\n\t"
    "v_mfma_f32_32x32x16_bf16 v[128:143], v[184:187], v[240:243], 0\n\t"
    "v_exp_f32 v150, v150\n\t"
    "v_exp_f32 v151, v151\n\t"
    "v_exp_f32 v152, v152\n\t"
    "v_cvt_pk_bf16_f32 v145, v146, v147\n\t"
    "v_cvt_pk_bf16_f32 v146, v148, v149\n\t"
    "v_mfma_f32_32x32x16_bf16 v[128:143], v[188:191], v[244:247], v[128:143]\n\t"
    "s_add_u32 m0, %[ldw], 0\n\t"
    "s_add_u32 s80, s60, 0\n\t"
    "buffer_load_dwordx4 v248, %[ro], s80 offen lds\n\t"
    "v_exp_f32 v153, v153\n\t"
    "v_exp_f32 v154, v154\n\t"
    "v_exp_f32 v155, v155\n\t"
    "v_exp_f32 v156, v156\n\t"
    "v_exp_f32 v157, v157\n\t"
    "v_exp_f32 v158, v158\n\t"
    "v_exp_f32 v159, v159\n\t"
    "v_cvt_pk_bf16_f32 v147, v150, v151\n\t"
    "v_cvt_pk_bf16_f32 v148, v152, v153\n\t"
    "v_cvt_pk_bf16_f32 v149, v154, v155\n\t"
    "v_cvt_pk_bf16_f32 v150, v156, v157\n\t"
    "v_cvt_pk_bf16_f32 v151, v158, v159\n\t"
    "v_mfma_f32_32x32x16_bf16 v[80:95], v[192:195], v[144:147], v[80:95]\n\t"
    "v_exp_f32 v128, v128\n\t"
    "v_exp_f32 v129, v129\n\t"
    "v_exp_f32 v130, v130\n\t"
    "v_mfma_f32_32x32x16_bf16 v[80:95], v[196:199], v[148:151], v[80:95]\n\t"
    "v_exp_f32 v131, v131\n\t"
    "v_exp_f32 v132, v132\n\t"
    "v_exp_f32 v133, v133\n\t"
    "v_cvt_pk_bf16_f32 v128, v128, v129\n\t"
    "v_mfma_f32_32x32x16_bf16 v[144:159], v[184:187], v[160:163], 0\n\t"
    "v_exp_f32 v134, v134\n\t"
    "v_exp_f32 v135, v135\n\t"
    "v_exp_f32 v136, v136\n\t"
    "v_cvt_pk_bf16_f32 v129, v130, v131\n\t"
    "v_cvt_pk_bf16_f32 v130, v132, v133\n\t"
    "v_mfma_f32_32x32x16_bf16 v[144:159], v[188:191], v[164:167], v[144:159]\n\t"
    "buffer_load_dwordx4 v[184:187], v252, %[rw], s52 offen sc1\n\t"
    "v_exp_f32 v137, v137\n\t"
    "v_exp_f32 v138, v138\n\t"
    "v_exp_f32 v139, v139\n\t"
    "v_exp_f32 v140, v140\n\t"
    "v_exp_f32 v141, v141\n\t"
    "v_exp_f32 v142, v142\n\t"
    "v_exp_f32 v143, v143\n\t"
    "v_cvt_pk_bf16_f32 v131, v134, v135\n\t"
    "v_cvt_pk_bf16_f32 v132, v136, v137\n\t"
    "v_cvt_pk_bf16_f32 v133, v138, v139\n\t"
    "v_cvt_pk_bf16_f32 v134, v140, v141\n\t"
    "v_cvt_pk_bf16_f32 v135, v142, v143\n\t"
    "v_mfma_f32_32x32x16_bf16 v[96:111], v[192:195], v[128:131], v[96:111]\n\t"
    "v_exp_f32 v144, v144\n\t"
    "v_exp_f32 v145, v145\n\t"
    "v_exp_f32 v146, v146\n\t"
    "v_mfma_f32_32x32x16_bf16 v[96:111], v[196:199], v[132:135], v[96:111]\n\t"
    "buffer_load_dwordx4 v[188:191], v252, %[rw], s52 offen offset:1024 sc1\n\t"
    "v_exp_f32 v147, v147\n\t"
    "v_exp_f32 v148, v148\n\t"
    "v_exp_f32 v149, v149\n\t"
    "v_cvt_pk_bf16_f32 v144, v144, v145\n\t"
    "s_waitcnt vmcnt(9)\n\t"
    "v_mfma_f32_32x32x16_bf16 v[128:143], v[168:171], v[224:227], 0\n\t"
    "v_exp_f32 v150, v150\n\t"
    "v_exp_f32 v151, v151\n\t"
    "v_exp_f32 v152, v152\n\t"
    "v_cvt_pk_bf16_f32 v145, v146, v147\n\t"
    "v_cvt_pk_bf16_f32 v146, v148, v149\n\t"
    "s_waitcnt vmcnt(7)\n\t"
    "v_mfma_f32_32x32x16_bf16 v[128:143], v[172:175], v[228:231], v[128:143]\n\t"
    "s_add_u32 m0, %[ldw], 8192\n\t"
    "s_add_u32 s80, s60, 57344\n\t"
    "buffer_load_dwordx4 v248, %[ro], s80 offen lds\n\t"
    "v_exp_f32 v153, v153\n\t"
    "v_exp_f32 v154, v154\n\t"
    "v_exp_f32 v155, v155\n\t"
    "v_exp_f32 v156, v156\n\t"
    "v_exp_f32 v157, v157\n\t"
    "v_exp_f32 v158, v158\n\t"
    "v_exp_f32 v159, v159\n\t"
    "v_cvt_pk_bf16_f32 v147, v150, v151\n\t"
    "v_cvt_pk_bf16_f32 v148, v152, v153\n\t"
    "v_cvt_pk_bf16_f32 v149, v154, v155\n\t"
    "v_cvt_pk_bf16_f32 v150, v156, v157\n\t"
    "v_cvt_pk_bf16_f32 v151, v158, v159\n\t"
    "v_mfma_f32_32x32x16_bf16 v[112:127], v[192:195], v[144:147], v[112:127]\n\t"
    "ds_read_b64_tr_b16 v[216:217], v249 offset:16384\n\t"
    "ds_read_b64_tr_b16 v[218:219], v249 offset:17408\n\t"
    "ds_read_b64_tr_b16 v[220:221], v250 offset:16384\n\t"
    "ds_read_b64_tr_b16 v[222:223], v250 offset:17408\n\t"
    "v_exp_f32 v128, v128\n\t"
    "v_exp_f32 v129, v129\n\t"
    "v_exp_f32 v130, v130\n\t"
    "v_mfma_f32_32x32x16_bf16 v[112:127], v[196:199], v[148:151], v[112:127]\n\t"
    "s_add_u32 m0, s70, 0\n\t"
    "s_add_u32 s80, s59, 688128\n\t"
    "buffer_load_dwordx4 v248, %[rd], s80 offen lds\n\t"
    "v_exp_f32 v131, v131\n\t"
    "v_exp_f32 v132, v132\n\t"
    "v_exp_f32 v133, v133\n\t"
    "v_cvt_pk_bf16_f32 v128, v128, v129\n\t"
    "v_mfma_f32_32x32x16_bf16 v[144:159], v[168:171], v[232:235], 0\n\t"
    "v_exp_f32 v134, v134\n\t"
    "v_exp_f32 v135, v135\n\t"
    "v_exp_f32 v136, v136\n\t"
    "v_cvt_pk_bf16_f32 v129, v130, v131\n\t"
    "v_mfma_f32_32x32x16_bf16 v[144:159], v[172:175], v[236:239], v[144:159]\n\t"
    "s_add_u32 s80, s59, 694272\n\t"
    "buffer_load_dwordx4 v248, %[rd], s80 offen offset:1024 lds\n\t"
    "v_exp_f32 v137, v137\n\t"
    "v_exp_f32 v138, v138\n\t"
    "v_cvt_pk_bf16_f32 v130, v132, v133\n\t"
    "v_cvt_pk_bf16_f32 v131, v134, v135\n\t"
    "v_cvt_pk_bf16_f32 v132, v136, v137\n\t"
    "s_waitcnt lgkmcnt(0)\n\t"
    "v_mfma_f32_32x32x16_bf16 v[0:15], v[216:219], v[200:203], 0\n\t"
    "buffer_load_dwordx4 v[192:195], v252, %[rw], s52 offen offset:2048 sc1\n\t"
    "v_exp_f32 v139, v139\n\t"
    "v_exp_f32 v140, v140\n\t"
    "v_exp_f32 v141, v141\n\t"
    "v_cvt_pk_bf16_f32 v133, v138, v139\n\t"
    "v_mfma_f32_32x32x16_bf16 v[16:31], v[220:223], v[200:203], 0\n\t"
    "s_add_u32 s57, s57, 4096\n\t"
    "buffer_load_dwordx4 v[200:203], v252, %[rw], s57 offen sc1\n\t"
    "v_exp_f32 v142, v142\n\t"
    "v_exp_f32 v143, v143\n\t"
    "v_cvt_pk_bf16_f32 v134, v140, v141\n\t"
    "v_cvt_pk_bf16_f32 v135, v142, v143\n\t"
    "s_waitcnt vmcnt(10)\n\t"
    "s_barrier\n\t"
    "s_cmp_eq_u32 %[wv], 0\n\t"
    "s_cbranch_scc0 Lpubpb1_%=\n\t"
    "s_mov_b64 exec, 1\n\t"
    "s_add_u32 s80, s71, 65536\n\t"
    "v_mov_b32 v254, s84\n\t"
    "v_mov_b32 v255, s85\n\t"
    "buffer_store_dwordx2 v[254:255], off, %[rw], s80 sc1\n\t"
    "s_mov_b64 exec, -1\n\t"
    "Lpubpb1_%=:\n\t"
    "s_cmp_eq_u32 %[wv], 1\n\t"
    "s_cbranch_scc0 Lpubpb2_%=\n\t"
    "s_mov_b64 exec, 1\n\t"
    "s_add_u32 s80, s67, 32768\n\t"
    "v_mov_b32 v254, s84\n\t"
    "v_mov_b32 v255, s85\n\t"
    "buffer_store_dwordx2 v[254:255], off, %[rw], s80 sc1\n\t"
    "s_mov_b64 exec, -1\n\t"
    "Lpubpb2_%=:\n\t"
    "s_waitcnt vmcnt(9)\n\t"
    "v_mfma_f32_32x32x16_bf16 v[64:79], v[176:179], v[128:131], v[64:79]\n\t"
    "v_exp_f32 v144, v144\n\t"
    "v_exp_f32 v145, v145\n\t"
    "v_exp_f32 v146, v146\n\t"
    "s_waitcnt vmcnt(8)\n\t"
    "v_mfma_f32_32x32x16_bf16 v[64:79], v[180:183], v[132:135], v[64:79]\n\t"
    "buffer_load_dwordx4 v[196:199], v252, %[rw], s52 offen offset:3072 sc1\n\t"
    "v_exp_f32 v147, v147\n\t"
    "v_exp_f32 v148, v148\n\t"
    "v_exp_f32 v149, v149\n\t"
    "v_cvt_pk_bf16_f32 v144, v144, v145\n\t"
    "v_mfma_f32_32x32x16_bf16 v[128:143], v[168:171], v[240:243], 0\n\t"
    "v_exp_f32 v150, v150\n\t"
    "v_exp_f32 v151, v151\n\t"
    "v_exp_f32 v152, v152\n\t"
    "v_cvt_pk_bf16_f32 v145, v146, v147\n\t"
    "v_mfma_f32_32x32x16_bf16 v[128:143], v[172:175], v[244:247], v[128:143]\n\t"
    "s_add_u32 m0, %[ldw], 16384\n\t"
    "s_add_u32 s80, s60, 114688\n\t"
    "buffer_load_dwordx4 v248, %[ro], s80 offen lds\n\t"
    "v_exp_f32 v153, v153\n\t"
    "v_exp_f32 v154, v154\n\t"
    "v_cvt_pk_bf16_f32 v146, v148, v149\n\t"
    "v_cvt_pk_bf16_f32 v147, v150, v151\n\t"
    "v_cvt_pk_bf16_f32 v148, v152, v153\n\t"
    "v_mfma_f32_32x32x16_bf16 v[32:47], v[216:219], v[204:207], 0\n\t"
    "v_exp_f32 v155, v155\n\t"
    "v_exp_f32 v156, v156\n\t"
    "v_exp_f32 v157, v157\n\t"
    "v_cvt_pk_bf16_f32 v149, v154, v155\n\t"
    "v_mfma_f32_32x32x16_bf16 v[48:63], v[220:223], v[204:207], 0\n\t"
    "buffer_load_dwordx4 v[204:207], v252, %[rw], s57 offen offset:1024 sc1\n\t"
    "v_exp_f32 v158, v158\n\t"
    "v_exp_f32 v159, v159\n\t"
    "v_cvt_pk_bf16_f32 v150, v156, v157\n\t"
    "v_cvt_pk_bf16_f32 v151, v158, v159\n\t"
    "v_mfma_f32_32x32x16_bf16 v[80:95], v[176:179], v[144:147], v[80:95]\n\t"
    "ds_read_b64_tr_b16 v[216:217], v249 offset:32768\n\t"
    "ds_read_b64_tr_b16 v[218:219], v249 offset:33792\n\t"
    "ds_read_b64_tr_b16 v[220:221], v250 offset:32768\n\t"
    "ds_read_b64_tr_b16 v[222:223], v250 offset:33792\n\t"
    "v_exp_f32 v128, v128\n\t"
    "v_exp_f32 v129, v129\n\t"
    "v_exp_f32 v130, v130\n\t"
    "v_mfma_f32_32x32x16_bf16 v[80:95], v[180:183], v[148:151], v[80:95]\n\t"
    "s_add_u32 m0, s70, 16384\n\t"
    "s_add_u32 s80, s59, 917504\n\t"
    "buffer_load_dwordx4 v248, %[rd], s80 offen lds\n\t"
    "v_exp_f32 v131, v131\n\t"
    "v_exp_f32 v132, v132\n\t"
    "v_exp_f32 v133, v133\n\t"
    "v_cvt_pk_bf16_f32 v128, v128, v129\n\t"
    "v_mfma_f32_32x32x16_bf16 v[144:159], v[168:171], v[160:163], 0\n\t"
    "v_exp_f32 v134, v134\n\t"
    "v_exp_f32 v135, v135\n\t"
    "v_exp_f32 v136, v136\n\t"
    "v_cvt_pk_bf16_f32 v129, v130, v131\n\t"
    "v_mfma_f32_32x32x16_bf16 v[144:159], v[172:175], v[164:167], v[144:159]\n\t"
    "s_add_u32 s80, s59, 923648\n\t"
    "buffer_load_dwordx4 v248, %[rd], s80 offen offset:1024 lds\n\t"
    "v_exp_f32 v137, v137\n\t"
    "v_exp_f32 v138, v138\n\t"
    "v_cvt_pk_bf16_f32 v130, v132, v133\n\t"
    "v_cvt_pk_bf16_f32 v131, v134, v135\n\t"
    "v_cvt_pk_bf16_f32 v132, v136, v137\n\t"
    "s_waitcnt lgkmcnt(0)\n\t"
    "v_mfma_f32_32x32x16_bf16 v[0:15], v[216:219], v[208:211], v[0:15]\n\t"
    "buffer_load_dwordx4 v[168:171], v252, %[rw], s53 offen sc1\n\t"
    "v_exp_f32 v139, v139\n\t"
    "v_exp_f32 v140, v140\n\t"
    "v_exp_f32 v141, v141\n\t"
    "v_cvt_pk_bf16_f32 v133, v138, v139\n\t"
    "v_mfma_f32_32x32x16_bf16 v[16:31], v[220:223], v[208:211], v[16:31]\n\t"
    "buffer_load_dwordx4 v[208:211], v252, %[rw], s57 offen offset:2048 sc1\n\t"
    "v_exp_f32 v142, v142\n\t"
    "v_exp_f32 v143, v143\n\t"
    "v_cvt_pk_bf16_f32 v134, v140, v141\n\t"
    "v_cvt_pk_bf16_f32 v135, v142, v143\n\t"
    "v_mfma_f32_32x32x16_bf16 v[96:111], v[176:179], v[128:131], v[96:111]\n\t"
    "v_exp_f32 v144, v144\n\t"
    "v_exp_f32 v145, v145\n\t"
    "v_exp_f32 v146, v146\n\t"
    "v_mfma_f32_32x32x16_bf16 v[96:111], v[180:183], v[132:135], v[96:111]\n\t"
    "buffer_load_dwordx4 v[172:175], v252, %[rw], s53 offen offset:1024 sc1\n\t"
    "v_exp_f32 v147, v147\n\t"
    "v_exp_f32 v148, v148\n\t"
    "v_exp_f32 v149, v149\n\t"
    "v_cvt_pk_bf16_f32 v144, v144, v145\n\t"
    "s_waitcnt vmcnt(14)\n\t"
    "v_mfma_f32_32x32x16_bf16 v[128:143], v[184:187], v[224:227], 0\n\t"
    "v_exp_f32 v150, v150\n\t"
    "v_exp_f32 v151, v151\n\t"
    "v_exp_f32 v152, v152\n\t"
    "v_cvt_pk_bf16_f32 v145, v146, v147\n\t"
    "s_waitcnt vmcnt(13)\n\t"
    "v_mfma_f32_32x32x16_bf16 v[128:143], v[188:191], v[228:231], v[128:143]\n\t"
    "v_exp_f32 v153, v153\n\t"
    "v_exp_f32 v154, v154\n\t"
    "v_cvt_pk_bf16_f32 v146, v148, v149\n\t"
    "v_cvt_pk_bf16_f32 v147, v150, v151\n\t"
    "v_cvt_pk_bf16_f32 v148, v152, v153\n\t"
    "v_mfma_f32_32x32x16_bf16 v[32:47], v[216:219], v[212:215], v[32:47]\n\t"
    "v_exp_f32 v155, v155\n\t"
    "v_exp_f32 v156, v156\n\t"
    "v_exp_f32 v157, v157\n\t"
    "v_cvt_pk_bf16_f32 v149, v154, v155\n\t"
    "v_mfma_f32_32x32x16_bf16 v[48:63], v[220:223], v[212:215], v[48:63]\n\t"
    "buffer_load_dwordx4 v[212:215], v252, %[rw], s57 offen offset:3072 sc1\n\t"
    "v_exp_f32 v158, v158\n\t"
    "v_exp_f32 v159, v159\n\t"
    "v_cvt_pk_bf16_f32 v150, v156, v157\n\t"
    "v_cvt_pk_bf16_f32 v151, v158, v159\n\t"
    "v_mfma_f32_32x32x16_bf16 v[112:127], v[176:179], v[144:147], v[112:127]\n\t"
    "ds_read_b64_tr_b16 v[216:217], v249 offset:49152\n\t"
    "ds_read_b64_tr_b16 v[218:219], v249 offset:50176\n\t"
    "ds_read_b64_tr_b16 v[220:221], v250 offset:49152\n\t"
    "ds_read_b64_tr_b16 v[222:223], v250 offset:50176\n\t"
    "v_exp_f32 v128, v128\n\t"
    "v_exp_f32 v129, v129\n\t"
    "v_exp_f32 v130, v130\n\t"
    "v_mfma_f32_32x32x16_bf16 v[112:127], v[180:183], v[148:151], v[112:127]\n\t"
    "s_add_u32 m0, s70, 32768\n\t"
    "s_add_u32 s80, s59, 1146880\n\t"
    "buffer_load_dwordx4 v248, %[rd], s80 offen lds\n\t"
    "v_exp_f32 v131, v131\n\t"
    "v_exp_f32 v132, v132\n\t"
    "v_exp_f32 v133, v133\n\t"
    "v_cvt_pk_bf16_f32 v128, v128, v129\n\t"
    "v_mfma_f32_32x32x16_bf16 v[144:159], v[184:187], v[232:235], 0\n\t"
    "v_exp_f32 v134, v134\n\t"
    "v_exp_f32 v135, v135\n\t"
    "v_exp_f32 v136, v136\n\t"
    "v_cvt_pk_bf16_f32 v129, v130, v131\n\t"
    "v_mfma_f32_32x32x16_bf16 v[144:159], v[188:191], v[236:239], v[144:159]\n\t"
    "s_add_u32 s80, s59, 1153024\n\t"
    "buffer_load_dwordx4 v248, %[rd], s80 offen offset:1024 lds\n\t"
    "v_exp_f32 v137, v137\n\t"
    "v_exp_f32 v138, v138\n\t"
    "v_cvt_pk_bf16_f32 v130, v132, v133\n\t"
    "v_cvt_pk_bf16_f32 v131, v134, v135\n\t"
    "v_cvt_pk_bf16_f32 v132, v136, v137\n\t"
    "s_waitcnt vmcnt(11) lgkmcnt(0)\n\t"
    "v_mfma_f32_32x32x16_bf16 v[0:15], v[216:219], v[200:203], v[0:15]\n\t"
    "buffer_load_dwordx4 v[176:179], v252, %[rw], s53 offen offset:2048 sc1\n\t"
    "v_exp_f32 v139, v139\n\t"
    "v_exp_f32 v140, v140\n\t"
    "v_exp_f32 v141, v141\n\t"
    "v_cvt_pk_bf16_f32 v133, v138, v139\n\t"
    "v_mfma_f32_32x32x16_bf16 v[16:31], v[220:223], v[200:203], v[16:31]\n\t"
    "s_add_u32 s57, s57, 4096\n\t"
    "buffer_load_dwordx4 v[200:203], v252, %[rw], s57 offen sc1\n\t"
    "v_exp_f32 v142, v142\n\t"
    "v_exp_f32 v143, v143\n\t"
    "v_cvt_pk_bf16_f32 v134, v140, v141\n\t"
    "v_cvt_pk_bf16_f32 v135, v142, v143\n\t"
    "v_mfma_f32_32x32x16_bf16 v[64:79], v[192:195], v[128:131], v[64:79]\n\t"
    "v_exp_f32 v144, v144\n\t"
    "v_exp_f32 v145, v145\n\t"
    "v_exp_f32 v146, v146\n\t"
    "s_waitcnt vmcnt(12)\n\t"
    "v_mfma_f32_32x32x16_bf16 v[64:79], v[196:199], v[132:135], v[64:79]\n\t"
    "buffer_load_dwordx4 v[180:183], v252, %[rw], s53 offen offset:3072 sc1\n\t"
    "v_exp_f32 v147, v147\n\t"
    "v_exp_f32 v148, v148\n\t"
    "v_exp_f32 v149, v149\n\t"
    "v_cvt_pk_bf16_f32 v144, v144, v145\n\t"
    "v_mfma_f32_32x32x16_bf16 v[128:143], v[184:187], v[240:243], 0\n\t"
    "v_exp_f32 v150, v150\n\t"
    "v_exp_f32 v151, v151\n\t"
    "v_exp_f32 v152, v152\n\t"
    "v_cvt_pk_bf16_f32 v145, v146, v147\n\t"
    "v_mfma_f32_32x32x16_bf16 v[128:143], v[188:191], v[244:247], v[128:143]\n\t"
    "s_add_u32 m0, %[ldw], 24576\n\t"
    "s_add_u32 s80, s60, 172032\n\t"
    "buffer_load_dwordx4 v248, %[ro], s80 offen lds\n\t"
    "v_exp_f32 v153, v153\n\t"
    "v_exp_f32 v154, v154\n\t"
    "v_cvt_pk_bf16_f32 v146, v148, v149\n\t"
    "v_cvt_pk_bf16_f32 v147, v150, v151\n\t"
    "v_cvt_pk_bf16_f32 v148, v152, v153\n\t"
    "s_waitcnt vmcnt(12)\n\t"
    "v_mfma_f32_32x32x16_bf16 v[32:47], v[216:219], v[204:207], v[32:47]\n\t"
    "v_exp_f32 v155, v155\n\t"
    "v_exp_f32 v156, v156\n\t"
    "v_exp_f32 v157, v157\n\t"
    "v_cvt_pk_bf16_f32 v149, v154, v155\n\t"
    "v_mfma_f32_32x32x16_bf16 v[48:63], v[220:223], v[204:207], v[48:63]\n\t"
    "buffer_load_dwordx4 v[204:207], v252, %[rw], s57 offen offset:1024 sc1\n\t"
    "v_exp_f32 v158, v158\n\t"
    "v_exp_f32 v159, v159\n\t"
    "v_cvt_pk_bf16_f32 v150, v156, v157\n\t"
    "v_cvt_pk_bf16_f32 v151, v158, v159\n\t"
    "v_mfma_f32_32x32x16_bf16 v[80:95], v[192:195], v[144:147], v[80:95]\n\t"
    "ds_read_b64_tr_b16 v[216:217], v249\n\t"
    "ds_read_b64_tr_b16 v[218:219], v249 offset:1024\n\t"
    "ds_read_b64_tr_b16 v[220:221], v250\n\t"
    "ds_read_b64_tr_b16 v[222:223], v250 offset:1024\n\t"
    "v_exp_f32 v128, v128\n\t"
    "v_exp_f32 v129, v129\n\t"
    "v_exp_f32 v130, v130\n\t"
    "v_mfma_f32_32x32x16_bf16 v[80:95], v[196:199], v[148:151], v[80:95]\n\t"
    "s_add_u32 m0, s70, 49152\n\t"
    "s_add_u32 s80, s59, 1376256\n\t"
    "buffer_load_dwordx4 v248, %[rd], s80 offen lds\n\t"
    "v_exp_f32 v131, v131\n\t"
    "v_exp_f32 v132, v132\n\t"
    "v_exp_f32 v133, v133\n\t"
    "v_cvt_pk_bf16_f32 v128, v128, v129\n\t"
    "v_mfma_f32_32x32x16_bf16 v[144:159], v[184:187], v[160:163], 0\n\t"
    "v_exp_f32 v134, v134\n\t"
    "v_exp_f32 v135, v135\n\t"
    "v_exp_f32 v136, v136\n\t"
    "v_cvt_pk_bf16_f32 v129, v130, v131\n\t"
    "v_mfma_f32_32x32x16_bf16 v[144:159], v[188:191], v[164:167], v[144:159]\n\t"
    "s_add_u32 s80, s59, 1382400\n\t"
    "buffer_load_dwordx4 v248, %[rd], s80 offen offset:1024 lds\n\t"
    "v_exp_f32 v137, v137\n\t"
    "v_exp_f32 v138, v138\n\t"
    "v_cvt_pk_bf16_f32 v130, v132, v133\n\t"
    "v_cvt_pk_bf16_f32 v131, v134, v135\n\t"
    "v_cvt_pk_bf16_f32 v132, v136, v137\n\t"
    "s_waitcnt vmcnt(11) lgkmcnt(0)\n\t"
    "v_mfma_f32_32x32x16_bf16 v[0:15], v[216:219], v[208:211], v[0:15]\n\t"
    "buffer_load_dwordx4 v[184:187], v252, %[rw], s46 offen sc1\n\t"
    "v_exp_f32 v139, v139\n\t"
    "v_exp_f32 v140, v140\n\t"
    "v_exp_f32 v141, v141\n\t"
    "v_cvt_pk_bf16_f32 v133, v138, v139\n\t"
    "v_mfma_f32_32x32x16_bf16 v[16:31], v[220:223], v[208:211], v[16:31]\n\t"
    "buffer_load_dwordx4 v[208:211], v252, %[rw], s57 offen offset:2048 sc1\n\t"
    "v_exp_f32 v142, v142\n\t"
    "v_exp_f32 v143, v143\n\t"
    "v_cvt_pk_bf16_f32 v134, v140, v141\n\t"
    "v_cvt_pk_bf16_f32 v135, v142, v143\n\t"
    "v_mfma_f32_32x32x16_bf16 v[96:111], v[192:195], v[128:131], v[96:111]\n\t"
    "v_exp_f32 v144, v144\n\t"
    "v_exp_f32 v145, v145\n\t"
    "v_exp_f32 v146, v146\n\t"
    "v_mfma_f32_32x32x16_bf16 v[96:111], v[196:199], v[132:135], v[96:111]\n\t"
    "buffer_load_dwordx4 v[188:191], v252, %[rw], s46 offen offset:1024 sc1\n\t"
    "v_exp_f32 v147, v147\n\t"
    "v_exp_f32 v148, v148\n\t"
    "v_exp_f32 v149, v149\n\t"
    "v_cvt_pk_bf16_f32 v144, v144, v145\n\t"
    "v_mfma_f32_32x32x16_bf16 v[128:143], v[168:171], v[224:227], 0\n\t"
    "v_exp_f32 v150, v150\n\t"
    "v_exp_f32 v151, v151\n\t"
    "v_exp_f32 v152, v152\n\t"
    "v_cvt_pk_bf16_f32 v145, v146, v147\n\t"
    "s_waitcnt vmcnt(13)\n\t"
    "v_mfma_f32_32x32x16_bf16 v[128:143], v[172:175], v[228:231], v[128:143]\n\t"
    "s_add_u32 m0, %[ldw], 32768\n\t"
    "s_add_u32 s80, s60, 229376\n\t"
    "buffer_load_dwordx4 v248, %[ro], s80 offen lds\n\t"
    "v_exp_f32 v153, v153\n\t"
    "v_exp_f32 v154, v154\n\t"
    "v_cvt_pk_bf16_f32 v146, v148, v149\n\t"
    "v_cvt_pk_bf16_f32 v147, v150, v151\n\t"
    "v_cvt_pk_bf16_f32 v148, v152, v153\n\t"
    "s_waitcnt vmcnt(13)\n\t"
    "v_mfma_f32_32x32x16_bf16 v[32:47], v[216:219], v[212:215], v[32:47]\n\t"
    "v_exp_f32 v155, v155\n\t"
    "v_exp_f32 v156, v156\n\t"
    "v_exp_f32 v157, v157\n\t"
    "v_cvt_pk_bf16_f32 v149, v154, v155\n\t"
    "v_mfma_f32_32x32x16_bf16 v[48:63], v[220:223], v[212:215], v[48:63]\n\t"
    "buffer_load_dwordx4 v[212:215], v252, %[rw], s57 offen offset:3072 sc1\n\t"
    "v_exp_f32 v158, v158\n\t"
    "v_exp_f32 v159, v159\n\t"
    "v_cvt_pk_bf16_f32 v150, v156, v157\n\t"
    "v_cvt_pk_bf16_f32 v151, v158, v159\n\t"
    "s_mov_b32 s80, 65536\n\t"
    "buffer_load_dwordx2 v[254:255], v251, %[rw], s80 offen sc1\n\t"
    "v_mfma_f32_32x32x16_bf16 v[112:127], v[192:195], v[144:147], v[112:127]\n\t"
    "ds_read_b64_tr_b16 v[216:217], v249 offset:16384\n\t"
    "ds_read_b64_tr_b16 v[218:219], v249 offset:17408\n\t"
    "ds_read_b64_tr_b16 v[220:221], v250 offset:16384\n\t"
    "ds_read_b64_tr_b16 v[222:223], v250 offset:17408\n\t"
    "v_exp_f32 v128, v128\n\t"
    "v_exp_f32 v129, v129\n\t"
    "v_exp_f32 v130, v130\n\t"
    "v_mfma_f32_32x32x16_bf16 v[112:127], v[196:199], v[148:151], v[112:127]\n\t"
    "s_add_u32 m0, s70, 0\n\t"
    "s_add_u32 s80, s59, 57344\n\t"
    "buffer_load_dwordx4 v248, %[rd], s80 offen lds\n\t"
    "v_exp_f32 v131, v131\n\t"
    "v_exp_f32 v132, v132\n\t"
    "v_exp_f32 v133, v133\n\t"
    "v_cvt_pk_bf16_f32 v128, v128, v129\n\t"
    "v_mfma_f32_32x32x16_bf16 v[144:159], v[168:171], v[232:235], 0\n\t"
    "v_exp_f32 v134, v134\n\t"
    "v_exp_f32 v135, v135\n\t"
    "v_exp_f32 v136, v136\n\t"
    "v_cvt_pk_bf16_f32 v129, v130, v131\n\t"
    "v_mfma_f32_32x32x16_bf16 v[144:159], v[172:175], v[236:239], v[144:159]\n\t"
    "s_add_u32 s80, s59, 63488\n\t"
    "buffer_load_dwordx4 v248, %[rd], s80 offen offset:1024 lds\n\t"
    "v_exp_f32 v137, v137\n\t"
    "v_exp_f32 v138, v138\n\t"
    "v_cvt_pk_bf16_f32 v130, v132, v133\n\t"
    "v_cvt_pk_bf16_f32 v131, v134, v135\n\t"
    "v_cvt_pk_bf16_f32 v132, v136, v137\n\t"
    "s_waitcnt vmcnt(13) lgkmcnt(0)\n\t"
    "v_mfma_f32_32x32x16_bf16 v[0:15], v[216:219], v[200:203], v[0:15]\n\t"
    "buffer_load_dwordx4 v[192:195], v252, %[rw], s46 offen offset:2048 sc1\n\t"
    "v_exp_f32 v139, v139\n\t"
    "v_exp_f32 v140, v140\n\t"
    "v_exp_f32 v141, v141\n\t"
    "v_cvt_pk_bf16_f32 v133, v138, v139\n\t"
    "v_mfma_f32_32x32x16_bf16 v[16:31], v[220:223], v[200:203], v[16:31]\n\t"
    "s_add_u32 s57, s57, 4096\n\t"
    "buffer_load_dwordx4 v[200:203], v252, %[rw], s57 offen sc1\n\t"
    "v_exp_f32 v142, v142\n\t"
    "v_exp_f32 v143, v143\n\t"
    "v_cvt_pk_bf16_f32 v134, v140, v141\n\t"
    "v_cvt_pk_bf16_f32 v135, v142, v143\n\t"
    "s_cmp_lt_u32 %[wv], 4\n\t"
    "s_cbranch_scc1 Lpr17_%=\n\t"
    "s_setprio 1\n\t"
    "Lpr17_%=:\n\t"
    "v_mfma_f32_32x32x16_bf16 v[64:79], v[176:179], v[128:131], v[64:79]\n\t"
    "v_exp_f32 v144, v144\n\t"
    "v_exp_f32 v145, v145\n\t"
    "v_exp_f32 v146, v146\n\t"
    "s_waitcnt vmcnt(14)\n\t"
    "v_mfma_f32_32x32x16_bf16 v[64:79], v[180:183], v[132:135], v[64:79]\n\t"
    "buffer_load_dwordx4 v[196:199], v252, %[rw], s46 offen offset:3072 sc1\n\t"
    "v_exp_f32 v147, v147\n\t"
    "v_exp_f32 v148, v148\n\t"
    "v_exp_f32 v149, v149\n\t"
    "v_cvt_pk_bf16_f32 v144, v144, v145\n\t"
    "v_mfma_f32_32x32x16_bf16 v[128:143], v[168:171], v[240:243], 0\n\t"
    "v_exp_f32 v150, v150\n\t"
    "v_exp_f32 v151, v151\n\t"
    "v_exp_f32 v152, v152\n\t"
    "v_cvt_pk_bf16_f32 v145, v146, v147\n\t"
    "v_mfma_f32_32x32x16_bf16 v[128:143], v[172:175], v[244:247], v[128:143]\n\t"
    "s_add_u32 m0, %[ldw], 40960\n\t"
    "s_add_u32 s80, s60, 286720\n\t"
    "buffer_load_dwordx4 v248, %[ro], s80 offen lds\n\t"
    "v_exp_f32 v153, v153\n\t"
    "v_exp_f32 v154, v154\n\t"
    "v_cvt_pk_bf16_f32 v146, v148, v149\n\t"
    "v_cvt_pk_bf16_f32 v147, v150, v151\n\t"
    "v_cvt_pk_bf16_f32 v148, v152, v153\n\t"
    "s_waitcnt vmcnt(14)\n\t"
    "v_mfma_f32_32x32x16_bf16 v[32:47], v[216:219], v[204:207], v[32:47]\n\t"
    "v_exp_f32 v155, v155\n\t"
    "v_exp_f32 v156, v156\n\t"
    "v_exp_f32 v157, v157\n\t"
    "v_cvt_pk_bf16_f32 v149, v154, v155\n\t"
    "v_mfma_f32_32x32x16_bf16 v[48:63], v[220:223], v[204:207], v[48:63]\n\t"
    "buffer_load_dwordx4 v[204:207], v252, %[rw], s57 offen offset:1024 sc1\n\t"
    "v_exp_f32 v158, v158\n\t"
    "v_exp_f32 v159, v159\n\t"
    "v_cvt_pk_bf16_f32 v150, v156, v157\n\t"
    "v_cvt_pk_bf16_f32 v151, v158, v159\n\t"
    "s_waitcnt vmcnt(7)\n\t"
    "s_mov_b32 s81, 0\n\t"
    "Lpb1_%=:\n\t"
    "v_cmp_eq_u64 vcc, s[84:85], v[254:255]\n\t"
    "s_cmp_eq_u64 vcc, -1\n\t"
    "s_cbranch_scc1 Lgb1_%=\n\t"
    "s_mov_b32 s80, 65536\n\t"
    "buffer_load_dwordx2 v[254:255], v251, %[rw], s80 offen sc1\n\t"
    "s_add_u32 s81, s81, 1\n\t"
    "s_waitcnt vmcnt(0)\n\t"
    "s_cmp_lt_u32 s81, 0x60000\n\t"
    "s_cbranch_scc1 Lpb1_%=\n\t"
    "Lgb1_%=:\n\t"
    "v_lshrrev_b32 v251, 7, v252\n\t"
    "v_lshlrev_b32 v251, 2, v251\n\t"
    "buffer_load_dword v251, v251, %[rw], s64 offen sc1\n\t"
    "v_mfma_f32_32x32x16_bf16 v[80:95], v[176:179], v[144:147], v[80:95]\n\t"
    "ds_read_b64_tr_b16 v[216:217], v249 offset:32768\n\t"
    "ds_read_b64_tr_b16 v[218:219], v249 offset:33792\n\t"
    "ds_read_b64_tr_b16 v[220:221], v250 offset:32768\n\t"
    "ds_read_b64_tr_b16 v[222:223], v250 offset:33792\n\t"
    "v_exp_f32 v128, v128\n\t"
    "v_exp_f32 v129, v129\n\t"
    "v_exp_f32 v130, v130\n\t"
    "v_mfma_f32_32x32x16_bf16 v[80:95], v[180:183], v[148:151], v[80:95]\n\t"
    "s_add_u32 m0, s70, 16384\n\t"
    "s_add_u32 s80, s59, 286720\n\t"
    "buffer_load_dwordx4 v248, %[rd], s80 offen lds\n\t"
    "v_exp_f32 v131, v131\n\t"
    "v_exp_f32 v132, v132\n\t"
    "v_exp_f32 v133, v133\n\t"
    "v_cvt_pk_bf16_f32 v128, v128, v129\n\t"
    "v_mfma_f32_32x32x16_bf16 v[144:159], v[168:171], v[160:163], 0\n\t"
    "v_exp_f32 v134, v134\n\t"
    "v_exp_f32 v135, v135\n\t"
    "v_exp_f32 v136, v136\n\t"
    "v_cvt_pk_bf16_f32 v129, v130, v131\n\t"
    "v_mfma_f32_32x32x16_bf16 v[144:159], v[172:175], v[164:167], v[144:159]\n\t"
    "s_add_u32 s80, s59, 292864\n\t"
    "buffer_load_dwordx4 v248, %[rd], s80 offen offset:1024 lds\n\t"
    "v_exp_f32 v137, v137\n\t"
    "v_exp_f32 v138, v138\n\t"
    "v_cvt_pk_bf16_f32 v130, v132, v133\n\t"
    "v_cvt_pk_bf16_f32 v131, v134, v135\n\t"
    "v_cvt_pk_bf16_f32 v132, v136, v137\n\t"
    "s_waitcnt lgkmcnt(0)\n\t"
    "v_mfma_f32_32x32x16_bf16 v[0:15], v[216:219], v[208:211], v[0:15]\n\t"
    "buffer_load_dwordx4 v[168:171], v252, %[rw], s47 offen sc1\n\t"
    "v_exp_f32 v139, v139\n\t"
    "v_exp_f32 v140, v140\n\t"
    "v_exp_f32 v141, v141\n\t"
    "v_cvt_pk_bf16_f32 v133, v138, v139\n\t"
    "v_mfma_f32_32x32x16_bf16 v[16:31], v[220:223], v[208:211], v[16:31]\n\t"
    "buffer_load_dwordx4 v[208:211], v252, %[rw], s57 offen offset:2048 sc1\n\t"
    "v_exp_f32 v142, v142\n\t"
    "v_exp_f32 v143, v143\n\t"
    "v_cvt_pk_bf16_f32 v134, v140, v141\n\t"
    "v_cvt_pk_bf16_f32 v135, v142, v143\n\t"
    "v_mfma_f32_32x32x16_bf16 v[96:111], v[176:179], v[128:131], v[96:111]\n\t"
    "v_exp_f32 v144, v144\n\t"
    "v_exp_f32 v145, v145\n\t"
    "v_exp_f32 v146, v146\n\t"
    "v_mfma_f32_32x32x16_bf16 v[96:111], v[180:183], v[132:135], v[96:111]\n\t"
    "buffer_load_dwordx4 v[172:175], v252, %[rw], s47 offen offset:1024 sc1\n\t"
    "v_exp_f32 v147, v147\n\t"
    "v_exp_f32 v148, v148\n\t"
    "v_exp_f32 v149, v149\n\t"
    "v_cvt_pk_bf16_f32 v144, v144, v145\n\t"
    "v_mfma_f32_32x32x16_bf16 v[128:143], v[184:187], v[224:227], 0\n\t"
    "v_exp_f32 v150, v150\n\t"
    "v_exp_f32 v151, v151\n\t"
    "v_exp_f32 v152, v152\n\t"
    "v_cvt_pk_bf16_f32 v145, v146, v147\n\t"
    "v_mfma_f32_32x32x16_bf16 v[128:143], v[188:191], v[228:231], v[128:143]\n\t"
    "v_exp_f32 v153, v153\n\t"
    "v_exp_f32 v154, v154\n\t"
    "v_cvt_pk_bf16_f32 v146, v148, v149\n\t"
    "v_cvt_pk_bf16_f32 v147, v150, v151\n\t"
    "v_cvt_pk_bf16_f32 v148, v152, v153\n\t"
    "v_mfma_f32_32x32x16_bf16 v[32:47], v[216:219], v[212:215], v[32:47]\n\t"
    "v_exp_f32 v155, v155\n\t"
    "v_exp_f32 v156, v156\n\t"
    "v_exp_f32 v157, v157\n\t"
    "v_cvt_pk_bf16_f32 v149, v154, v155\n\t"
    "v_mfma_f32_32x32x16_bf16 v[48:63], v[220:223], v[212:215], v[48:63]\n\t"
    "buffer_load_dwordx4 v[212:215], v252, %[rw], s57 offen offset:3072 sc1\n\t"
    "v_exp_f32 v158, v158\n\t"
    "v_exp_f32 v159, v159\n\t"
    "v_cvt_pk_bf16_f32 v150, v156, v157\n\t"
    "v_cvt_pk_bf16_f32 v151, v158, v159\n\t"
    "v_mfma_f32_32x32x16_bf16 v[112:127], v[176:179], v[144:147], v[112:127]\n\t"
    "ds_read_b64_tr_b16 v[216:217], v249 offset:49152\n\t"
    "ds_read_b64_tr_b16 v[218:219], v249 offset:50176\n\t"
    "ds_read_b64_tr_b16 v[220:221], v250 offset:49152\n\t"
    "ds_read_b64_tr_b16 v[222:223], v250 offset:50176\n\t"
    "v_exp_f32 v128, v128\n\t"
    "v_exp_f32 v129, v129\n\t"
    "v_exp_f32 v130, v130\n\t"
    "v_mfma_f32_32x32x16_bf16 v[112:127], v[180:183], v[148:151], v[112:127]\n\t"
    "s_add_u32 m0, s70, 32768\n\t"
    "s_add_u32 s80, s59, 516096\n\t"
    "buffer_load_dwordx4 v248, %[rd], s80 offen lds\n\t"
    "v_exp_f32 v131, v131\n\t"
    "v_exp_f32 v132, v132\n\t"
    "v_exp_f32 v133, v133\n\t"
    "v_cvt_pk_bf16_f32 v128, v128, v129\n\t"
    "v_mfma_f32_32x32x16_bf16 v[144:159], v[184:187], v[232:235], 0\n\t"
    "v_exp_f32 v134, v134\n\t"
    "v_exp_f32 v135, v135\n\t"
    "v_exp_f32 v136, v136\n\t"
    "v_cvt_pk_bf16_f32 v129, v130, v131\n\t"
    "v_mfma_f32_32x32x16_bf16 v[144:159], v[188:191], v[236:239], v[144:159]\n\t"
    "s_add_u32 s80, s59, 522240\n\t"
    "buffer_load_dwordx4 v248, %[rd], s80 offen offset:1024 lds\n\t"
    "v_exp_f32 v137, v137\n\t"
    "v_exp_f32 v138, v138\n\t"
    "v_cvt_pk_bf16_f32 v130, v132, v133\n\t"
    "v_cvt_pk_bf16_f32 v131, v134, v135\n\t"
    "v_cvt_pk_bf16_f32 v132, v136, v137\n\t"
    "s_waitcnt vmcnt(12) lgkmcnt(0)\n\t"
    "v_mfma_f32_32x32x16_bf16 v[0:15], v[216:219], v[200:203], v[0:15]\n\t"
    "buffer_load_dwordx4 v[176:179], v252, %[rw], s47 offen offset:2048 sc1\n\t"
    "v_exp_f32 v139, v139\n\t"
    "v_exp_f32 v140, v140\n\t"
    "v_exp_f32 v141, v141\n\t"
    "v_cvt_pk_bf16_f32 v133, v138, v139\n\t"
    "v_mfma_f32_32x32x16_bf16 v[16:31], v[220:223], v[200:203], v[16:31]\n\t"
    "s_add_u32 s57, s57, 4096\n\t"
    "buffer_load_dwordx4 v[200:203], v252, %[rw], s57 offen sc1\n\t"
    "v_exp_f32 v142, v142\n\t"
    "v_exp_f32 v143, v143\n\t"
    "v_cvt_pk_bf16_f32 v134, v140, v141\n\t"
    "v_cvt_pk_bf16_f32 v135, v142, v143\n\t"
    "v_mfma_f32_32x32x16_bf16 v[64:79], v[192:195], v[128:131], v[64:79]\n\t"
    "v_exp_f32 v144, v144\n\t"
    "v_exp_f32 v145, v145\n\t"
    "v_exp_f32 v146, v146\n\t"
    "s_waitcnt vmcnt(13)\n\t"
    "v_mfma_f32_32x32x16_bf16 v[64:79], v[196:199], v[132:135], v[64:79]\n\t"
    "buffer_load_dwordx4 v[180:183], v252, %[rw], s47 offen offset:3072 sc1\n\t"
    "v_exp_f32 v147, v147\n\t"
    "v_exp_f32 v148, v148\n\t"
    "v_exp_f32 v149, v149\n\t"
    "v_cvt_pk_bf16_f32 v144, v144, v145\n\t"
    "v_mfma_f32_32x32x16_bf16 v[128:143], v[184:187], v[240:243], 0\n\t"
    "v_exp_f32 v150, v150\n\t"
    "v_exp_f32 v151, v151\n\t"
    "v_exp_f32 v152, v152\n\t"
    "v_cvt_pk_bf16_f32 v145, v146, v147\n\t"
    "v_mfma_f32_32x32x16_bf16 v[128:143], v[188:191], v[244:247], v[128:143]\n\t"
    "s_add_u32 m0, %[ldw], 49152\n\t"
    "s_add_u32 s80, s60, 344064\n\t"
    "buffer_load_dwordx4 v248, %[ro], s80 offen lds\n\t"
    "v_exp_f32 v153, v153\n\t"
    "v_exp_f32 v154, v154\n\t"
    "v_cvt_pk_bf16_f32 v146, v148, v149\n\t"
    "v_cvt_pk_bf16_f32 v147, v150, v151\n\t"
    "v_cvt_pk_bf16_f32 v148, v152, v153\n\t"
    "s_waitcnt vmcnt(13)\n\t"
    "v_mfma_f32_32x32x16_bf16 v[32:47], v[216:219], v[204:207], v[32:47]\n\t"
    "v_exp_f32 v155, v155\n\t"
    "v_exp_f32 v156, v156\n\t"
    "v_exp_f32 v157, v157\n\t"
    "v_cvt_pk_bf16_f32 v149, v154, v155\n\t"
    "v_mfma_f32_32x32x16_bf16 v[48:63], v[220:223], v[204:207], v[48:63]\n\t"
    "buffer_load_dwordx4 v[204:207], v252, %[rw], s57 offen offset:1024 sc1\n\t"
    "v_exp_f32 v158, v158\n\t"
    "v_exp_f32 v159, v159\n\t"
    "v_cvt_pk_bf16_f32 v150, v156, v157\n\t"
    "v_cvt_pk_bf16_f32 v151, v158, v159\n\t"
    "v_mfma_f32_32x32x16_bf16 v[80:95], v[192:195], v[144:147], v[80:95]\n\t"
    "ds_read_b64_tr_b16 v[216:217], v249\n\t"
    "ds_read_b64_tr_b16 v[218:219], v249 offset:1024\n\t"
    "ds_read_b64_tr_b16 v[220:221], v250\n\t"
    "ds_read_b64_tr_b16 v[222:223], v250 offset:1024\n\t"
    "v_exp_f32 v128, v128\n\t"
    "v_exp_f32 v129, v129\n\t"
    "v_exp_f32 v130, v130\n\t"
    "v_mfma_f32_32x32x16_bf16 v[80:95], v[196:199], v[148:151], v[80:95]\n\t"
    "s_add_u32 m0, s70, 49152\n\t"
    "s_add_u32 s80, s59, 745472\n\t"
    "buffer_load_dwordx4 v248, %[rd], s80 offen lds\n\t"
    "v_exp_f32 v131, v131\n\t"
    "v_exp_f32 v132, v132\n\t"
    "v_exp_f32 v133, v133\n\t"
    "v_cvt_pk_bf16_f32 v128, v128, v129\n\t"
    "v_mfma_f32_32x32x16_bf16 v[144:159], v[184:187], v[160:163], 0\n\t"
    "v_exp_f32 v134, v134\n\t"
    "v_exp_f32 v135, v135\n\t"
    "v_exp_f32 v136, v136\n\t"
    "v_cvt_pk_bf16_f32 v129, v130, v131\n\t"
    "v_mfma_f32_32x32x16_bf16 v[144:159], v[188:191], v[164:167], v[144:159]\n\t"
    "s_add_u32 s80, s59, 751616\n\t"
    "buffer_load_dwordx4 v248, %[rd], s80 offen offset:1024 lds\n\t"
    "v_exp_f32 v137, v137\n\t"
    "v_exp_f32 v138, v138\n\t"
    "v_cvt_pk_bf16_f32 v130, v132, v133\n\t"
    "v_cvt_pk_bf16_f32 v131, v134, v135\n\t"
    "v_cvt_pk_bf16_f32 v132, v136, v137\n\t"
    "s_waitcnt vmcnt(11) lgkmcnt(0)\n\t"
    "v_mfma_f32_32x32x16_bf16 v[0:15], v[216:219], v[208:211], v[0:15]\n\t"
    "buffer_load_dwordx4 v[184:187], v252, %[rw], s48 offen sc1\n\t"
    "v_exp_f32 v139, v139\n\t"
    "v_exp_f32 v140, v140\n\t"
    "v_exp_f32 v141, v141\n\t"
    "v_cvt_pk_bf16_f32 v133, v138, v139\n\t"
    "v_mfma_f32_32x32x16_bf16 v[16:31], v[220:223], v[208:211], v[16:31]\n\t"
    "buffer_load_dwordx4 v[208:211], v252, %[rw], s57 offen offset:2048 sc1\n\t"
    "v_exp_f32 v142, v142\n\t"
    "v_exp_f32 v143, v143\n\t"
    "v_cvt_pk_bf16_f32 v134, v140, v141\n\t"
    "v_cvt_pk_bf16_f32 v135, v142, v143\n\t"
    "v_mfma_f32_32x32x16_bf16 v[96:111], v[192:195], v[128:131], v[96:111]\n\t"
    "v_exp_f32 v144, v144\n\t"
    "v_exp_f32 v145, v145\n\t"
    "v_exp_f32 v146, v146\n\t"
    "v_mfma_f32_32x32x16_bf16 v[96:111], v[196:199], v[132:135], v[96:111]\n\t"
    "buffer_load_dwordx4 v[188:191], v252, %[rw], s48 offen offset:1024 sc1\n\t"
    "v_exp_f32 v147, v147\n\t"
    "v_exp_f32 v148, v148\n\t"
    "v_exp_f32 v149, v149\n\t"
    "v_cvt_pk_bf16_f32 v144, v144, v145\n\t"
    "v_mfma_f32_32x32x16_bf16 v[128:143], v[168:171], v[224:227], 0\n\t"
    "v_exp_f32 v150, v150\n\t"
    "v_exp_f32 v151, v151\n\t"
    "v_exp_f32 v152, v152\n\t"
    "v_cvt_pk_bf16_f32 v145, v146, v147\n\t"
    "s_waitcnt vmcnt(13)\n\t"
    "v_mfma_f32_32x32x16_bf16 v[128:143], v[172:175], v[228:231], v[128:143]\n\t"
    "v_exp_f32 v153, v153\n\t"
    "v_exp_f32 v154, v154\n\t"
    "v_cvt_pk_bf16_f32 v146, v148, v149\n\t"
    "v_cvt_pk_bf16_f32 v147, v150, v151\n\t"
    "v_cvt_pk_bf16_f32 v148, v152, v153\n\t"
    "s_waitcnt vmcnt(12)\n\t"
    "v_mfma_f32_32x32x16_bf16 v[32:47], v[216:219], v[212:215], v[32:47]\n\t"
    "v_exp_f32 v155, v155\n\t"
    "v_exp_f32 v156, v156\n\t"
    "v_exp_f32 v157, v157\n\t"
    "v_cvt_pk_bf16_f32 v149, v154, v155\n\t"
    "v_mfma_f32_32x32x16_bf16 v[48:63], v[220:223], v[212:215], v[48:63]\n\t"
    "buffer_load_dwordx4 v[212:215], v252, %[rw], s57 offen offset:3072 sc1\n\t"
    "v_exp_f32 v158, v158\n\t"
    "v_exp_f32 v159, v159\n\t"
    "v_cvt_pk_bf16_f32 v150, v156, v157\n\t"
    "v_cvt_pk_bf16_f32 v151, v158, v159\n\t"
    "v_mfma_f32_32x32x16_bf16 v[112:127], v[192:195], v[144:147], v[112:127]\n\t"
    "ds_read_b64_tr_b16 v[216:217], v249 offset:16384\n\t"
    "ds_read_b64_tr_b16 v[218:219], v249 offset:17408\n\t"
    "ds_read_b64_tr_b16 v[220:221], v250 offset:16384\n\t"
    "ds_read_b64_tr_b16 v[222:223], v250 offset:17408\n\t"
    "v_exp_f32 v128, v128\n\t"
    "v_exp_f32 v129, v129\n\t"
    "v_exp_f32 v130, v130\n\t"
    "v_mfma_f32_32x32x16_bf16 v[112:127], v[196:199], v[148:151], v[112:127]\n\t"
    "s_add_u32 m0, s70, 0\n\t"
    "s_add_u32 s80, s59, 974848\n\t"
    "buffer_load_dwordx4 v248, %[rd], s80 offen lds\n\t"
    "v_exp_f32 v131, v131\n\t"
    "v_exp_f32 v132, v132\n\t"
    "v_exp_f32 v133, v133\n\t"
    "v_cvt_pk_bf16_f32 v128, v128, v129\n\t"
    "v_mfma_f32_32x32x16_bf16 v[144:159], v[168:171], v[232:235], 0\n\t"
    "v_exp_f32 v134, v134\n\t"
    "v_exp_f32 v135, v135\n\t"
    "v_exp_f32 v136, v136\n\t"
    "v_cvt_pk_bf16_f32 v129, v130, v131\n\t"
    "v_mfma_f32_32x32x16_bf16 v[144:159], v[172:175], v[236:239], v[144:159]\n\t"
    "s_add_u32 s80, s59, 980992\n\t"
    "buffer_load_dwordx4 v248, %[rd], s80 offen offset:1024 lds\n\t"
    "v_exp_f32 v137, v137\n\t"
    "v_exp_f32 v138, v138\n\t"
    "v_cvt_pk_bf16_f32 v130, v132, v133\n\t"
    "v_cvt_pk_bf16_f32 v131, v134, v135\n\t"
    "v_cvt_pk_bf16_f32 v132, v136, v137\n\t"
    "s_waitcnt vmcnt(11) lgkmcnt(0)\n\t"
    "v_mfma_f32_32x32x16_bf16 v[0:15], v[216:219], v[200:203], v[0:15]\n\t"
    "buffer_load_dwordx4 v[192:195], v252, %[rw], s48 offen offset:2048 sc1\n\t"
    "v_exp_f32 v139, v139\n\t"
    "v_exp_f32 v140, v140\n\t"
    "v_exp_f32 v141, v141\n\t"
    "v_cvt_pk_bf16_f32 v133, v138, v139\n\t"
    "v_mfma_f32_32x32x16_bf16 v[16:31], v[220:223], v[200:203], v[16:31]\n\t"
    "s_add_u32 s57, s57, 4096\n\t"
    "buffer_load_dwordx4 v[200:203], v252, %[rw], s57 offen sc1\n\t"
    "v_exp_f32 v142, v142\n\t"
    "v_exp_f32 v143, v143\n\t"
    "v_cvt_pk_bf16_f32 v134, v140, v141\n\t"
    "v_cvt_pk_bf16_f32 v135, v142, v143\n\t"
    "v_mfma_f32_32x32x16_bf16 v[64:79], v[176:179], v[128:131], v[64:79]\n\t"
    "v_exp_f32 v144, v144\n\t"
    "v_exp_f32 v145, v145\n\t"
    "v_exp_f32 v146, v146\n\t"
    "s_waitcnt vmcnt(12)\n\t"
    "v_mfma_f32_32x32x16_bf16 v[64:79], v[180:183], v[132:135], v[64:79]\n\t"
    "buffer_load_dwordx4 v[196:199], v252, %[rw], s48 offen offset:3072 sc1\n\t"
    "v_exp_f32 v147, v147\n\t"
    "v_exp_f32 v148, v148\n\t"
    "v_exp_f32 v149, v149\n\t"
    "v_cvt_pk_bf16_f32 v144, v144, v145\n\t"
    "v_mfma_f32_32x32x16_bf16 v[128:143], v[168:171], v[240:243], 0\n\t"
    "v_exp_f32 v150, v150\n\t"
    "v_exp_f32 v151, v151\n\t"
    "v_exp_f32 v152, v152\n\t"
    "v_cvt_pk_bf16_f32 v145, v146, v147\n\t"
    "v_mfma_f32_32x32x16_bf16 v[128:143], v[172:175], v[244:247], v[128:143]\n\t"
    "v_exp_f32 v153, v153\n\t"
    "v_exp_f32 v154, v154\n\t"
    "v_cvt_pk_bf16_f32 v146, v148, v149\n\t"
    "v_cvt_pk_bf16_f32 v147, v150, v151\n\t"
    "v_cvt_pk_bf16_f32 v148, v152, v153\n\t"
    "s_waitcnt vmcnt(11)\n\t"
    "v_mfma_f32_32x32x16_bf16 v[32:47], v[216:219], v[204:207], v[32:47]\n\t"
    "v_exp_f32 v155, v155\n\t"
    "v_exp_f32 v156, v156\n\t"
    "v_exp_f32 v157, v157\n\t"
    "v_cvt_pk_bf16_f32 v149, v154, v155\n\t"
    "v_mfma_f32_32x32x16_bf16 v[48:63], v[220:223], v[204:207], v[48:63]\n\t"
    "buffer_load_dwordx4 v[204:207], v252, %[rw], s57 offen offset:1024 sc1\n\t"
    "v_exp_f32 v158, v158\n\t"
    "v_exp_f32 v159, v159\n\t"
    "v_cvt_pk_bf16_f32 v150, v156, v157\n\t"
    "v_cvt_pk_bf16_f32 v151, v158, v159\n\t"
    "v_mfma_f32_32x32x16_bf16 v[80:95], v[176:179], v[144:147], v[80:95]\n\t"
    "ds_read_b64_tr_b16 v[216:217], v249 offset:32768\n\t"
    "ds_read_b64_tr_b16 v[218:219], v249 offset:33792\n\t"
    "ds_read_b64_tr_b16 v[220:221], v250 offset:32768\n\t"
    "ds_read_b64_tr_b16 v[222:223], v250 offset:33792\n\t"
    "v_exp_f32 v128, v128\n\t"
    "v_exp_f32 v129, v129\n\t"
    "v_exp_f32 v130, v130\n\t"
    "v_mfma_f32_32x32x16_bf16 v[80:95], v[180:183], v[148:151], v[80:95]\n\t"
    "s_add_u32 m0, s70, 16384\n\t"
    "s_add_u32 s80, s59, 1204224\n\t"
    "buffer_load_dwordx4 v248, %[rd], s80 offen lds\n\t"
    "v_exp_f32 v131, v131\n\t"
    "v_exp_f32 v132, v132\n\t"
    "v_exp_f32 v133, v133\n\t"
    "v_cvt_pk_bf16_f32 v128, v128, v129\n\t"
    "v_mfma_f32_32x32x16_bf16 v[144:159], v[168:171], v[160:163], 0\n\t"
    "v_exp_f32 v134, v134\n\t"
    "v_exp_f32 v135, v135\n\t"
    "v_exp_f32 v136, v136\n\t"
    "v_cvt_pk_bf16_f32 v129, v130, v131\n\t"
    "v_mfma_f32_32x32x16_bf16 v[144:159], v[172:175], v[164:167], v[144:159]\n\t"
    "s_add_u32 s80, s59, 1210368\n\t"
    "buffer_load_dwordx4 v248, %[rd], s80 offen offset:1024 lds\n\t"
    "v_exp_f32 v137, v137\n\t"
    "v_exp_f32 v138, v138\n\t"
    "v_cvt_pk_bf16_f32 v130, v132, v133\n\t"
    "v_cvt_pk_bf16_f32 v131, v134, v135\n\t"
    "v_cvt_pk_bf16_f32 v132, v136, v137\n\t"
    "s_waitcnt vmcnt(10) lgkmcnt(0)\n\t"
    "v_mfma_f32_32x32x16_bf16 v[0:15], v[216:219], v[208:211], v[0:15]\n\t"
    "v_exp_f32 v139, v139\n\t"
    "v_exp_f32 v140, v140\n\t"
    "v_exp_f32 v141, v141\n\t"
    "v_cvt_pk_bf16_f32 v133, v138, v139\n\t"
    "v_mfma_f32_32x32x16_bf16 v[16:31], v[220:223], v[208:211], v[16:31]\n\t"
    "buffer_load_dwordx4 v[208:211], v252, %[rw], s57 offen offset:2048 sc1\n\t"
    "v_exp_f32 v142, v142\n\t"
    "v_exp_f32 v143, v143\n\t"
    "v_cvt_pk_bf16_f32 v134, v140, v141\n\t"
    "v_cvt_pk_bf16_f32 v135, v142, v143\n\t"
    "v_mfma_f32_32x32x16_bf16 v[96:111], v[176:179], v[128:131], v[96:111]\n\t"
    "v_exp_f32 v144, v144\n\t"
    "v_exp_f32 v145, v145\n\t"
    "v_exp_f32 v146, v146\n\t"
    "v_mfma_f32_32x32x16_bf16 v[96:111], v[180:183], v[132:135], v[96:111]\n\t"
    "v_exp_f32 v147, v147\n\t"
    "v_exp_f32 v148, v148\n\t"
    "v_exp_f32 v149, v149\n\t"
    "v_cvt_pk_bf16_f32 v144, v144, v145\n\t"
    "v_mfma_f32_32x32x16_bf16 v[128:143], v[184:187], v[224:227], 0\n\t"
    "v_exp_f32 v150, v150\n\t"
    "v_exp_f32 v151, v151\n\t"
    "v_exp_f32 v152, v152\n\t"
    "v_cvt_pk_bf16_f32 v145, v146, v147\n\t"
    "s_waitcnt vmcnt(10)\n\t"
    "v_mfma_f32_32x32x16_bf16 v[128:143], v[188:191], v[228:231], v[128:143]\n\t"
    "v_exp_f32 v153, v153\n\t"
    "v_exp_f32 v154, v154\n\t"
    "v_cvt_pk_bf16_f32 v146, v148, v149\n\t"
    "v_cvt_pk_bf16_f32 v147, v150, v151\n\t"
    "v_cvt_pk_bf16_f32 v148, v152, v153\n\t"
    "s_waitcnt vmcnt(9)\n\t"
    "v_mfma_f32_32x32x16_bf16 v[32:47], v[216:219], v[212:215], v[32:47]\n\t"
    "v_exp_f32 v155, v155\n\t"
    "v_exp_f32 v156, v156\n\t"
    "v_exp_f32 v157, v157\n\t"
    "v_cvt_pk_bf16_f32 v149, v154, v155\n\t"
    "v_mfma_f32_32x32x16_bf16 v[48:63], v[220:223], v[212:215], v[48:63]\n\t"
    "buffer_load_dwordx4 v[212:215], v252, %[rw], s57 offen offset:3072 sc1\n\t"
    "v_exp_f32 v158, v158\n\t"
    "v_exp_f32 v159, v159\n\t"
    "v_cvt_pk_bf16_f32 v150, v156, v157\n\t"
    "v_cvt_pk_bf16_f32 v151, v158, v159\n\t"
    "v_mfma_f32_32x32x16_bf16 v[112:127], v[176:179], v[144:147], v[112:127]\n\t"
    "ds_read_b64_tr_b16 v[216:217], v249 offset:49152\n\t"
    "ds_read_b64_tr_b16 v[218:219], v249 offset:50176\n\t"
    "ds_read_b64_tr_b16 v[220:221], v250 offset:49152\n\t"
    "ds_read_b64_tr_b16 v[222:223], v250 offset:50176\n\t"
    "v_exp_f32 v128, v128\n\t"
    "v_exp_f32 v129, v129\n\t"
    "v_exp_f32 v130, v130\n\t"
    "v_mfma_f32_32x32x16_bf16 v[112:127], v[180:183], v[148:151], v[112:127]\n\t"
    "s_add_u32 m0, s70, 32768\n\t"
    "s_add_u32 s80, s59, 1433600\n\t"
    "buffer_load_dwordx4 v248, %[rd], s80 offen lds\n\t"
    "v_exp_f32 v131, v131\n\t"
    "v_exp_f32 v132, v132\n\t"
    "v_exp_f32 v133, v133\n\t"
    "v_cvt_pk_bf16_f32 v128, v128, v129\n\t"
    "v_mfma_f32_32x32x16_bf16 v[144:159], v[184:187], v[232:235], 0\n\t"
    "v_exp_f32 v134, v134\n\t"
    "v_exp_f32 v135, v135\n\t"
    "v_exp_f32 v136, v136\n\t"
    "v_cvt_pk_bf16_f32 v129, v130, v131\n\t"
    "v_mfma_f32_32x32x16_bf16 v[144:159], v[188:191], v[236:239], v[144:159]\n\t"
    "s_add_u32 s80, s59, 1439744\n\t"
    "buffer_load_dwordx4 v248, %[rd], s80 offen offset:1024 lds\n\t"
    "v_exp_f32 v137, v137\n\t"
    "v_exp_f32 v138, v138\n\t"
    "v_cvt_pk_bf16_f32 v130, v132, v133\n\t"
    "v_cvt_pk_bf16_f32 v131, v134, v135\n\t"
    "v_cvt_pk_bf16_f32 v132, v136, v137\n\t"
    "s_waitcnt vmcnt(8) lgkmcnt(0)\n\t"
    "v_mfma_f32_32x32x16_bf16 v[0:15], v[216:219], v[200:203], v[0:15]\n\t"
    "v_exp_f32 v139, v139\n\t"
    "v_exp_f32 v140, v140\n\t"
    "v_exp_f32 v141, v141\n\t"
    "v_cvt_pk_bf16_f32 v133, v138, v139\n\t"
    "v_mfma_f32_32x32x16_bf16 v[16:31], v[220:223], v[200:203], v[16:31]\n\t"
    "s_add_u32 s57, s57, 4096\n\t"
    "buffer_load_dwordx4 v[200:203], v252, %[rw], s57 offen sc1\n\t"
    "v_exp_f32 v142, v142\n\t"
    "v_exp_f32 v143, v143\n\t"
    "v_cvt_pk_bf16_f32 v134, v140, v141\n\t"
    "v_cvt_pk_bf16_f32 v135, v142, v143\n\t"
    "s_lshr_b32 s80, %[wv], 1\n\t"
    "s_lshl_b32 s80, s80, 14\n\t"
    "s_and_b32 s81, %[wv], 1\n\t"
    "s_lshl_b32 s81, s81, 8\n\t"
    "s_add_u32 s80, s80, s81\n\t"
    "s_add_u32 s80, s80, 65536\n\t"
    "s_mov_b32 s82, 0xaaaaaaaa\n\t"
    "s_mov_b32 s83, 0xaaaaaaaa\n\t"
    "v_lshrrev_b32 v181, 2, v252\n\t"
    "v_and_b32 v181, 124, v181\n\t"
    "v_and_b32 v180, 60, v181\n\t"
    "buffer_load_dword v180, v180, %[rw], s75 offen sc1\n\t"
    "buffer_load_dword v181, v181, %[rw], s63 offen sc1\n\t"
    "v_and_b32 v169, 0x300, v252\n\t"
    "v_and_b32 v182, 0xf0, v252\n\t"
    "v_lshl_or_b32 v182, v169, 1, v182\n\t"
    "v_and_b32 v169, 0x200, v252\n\t"
    "v_add_u32 v168, s80, v182\n\t"
    "v_lshl_add_u32 v168, v169, 1, v168\n\t"
    "v_mov_b32 v169, 0x3f80\n\t"
    "v_mov_b32 v170, s84\n\t"
    "v_mov_b32 v171, s85\n\t"
    "v_cndmask_b32 v169, v169, 1.0, s[82:83]\n\t"
    "s_mov_b32 s96, 0x0\n\t"
    "s_mov_b32 s97, 0xffff0000\n\t"
    "s_mov_b32 s86, 0x21800000\n\t"
    "s_mov_b32 s87, 0x5d800000\n\t"
    "s_mov_b32 s88, 0x7e800000\n\t"
    "v_mfma_f32_32x32x16_bf16 v[64:79], v[192:195], v[128:131], v[64:79]\n\t"
    "v_exp_f32 v144, v144\n\t"
    "v_exp_f32 v145, v145\n\t"
    "v_exp_f32 v146, v146\n\t"
    "s_waitcnt vmcnt(10)\n\t"
    "v_mfma_f32_32x32x16_bf16 v[64:79], v[196:199], v[132:135], v[64:79]\n\t"
    "v_exp_f32 v147, v147\n\t"
    "v_exp_f32 v148, v148\n\t"
    "v_exp_f32 v149, v149\n\t"
    "v_cvt_pk_bf16_f32 v144, v144, v145\n\t"
    "v_mfma_f32_32x32x16_bf16 v[128:143], v[184:187], v[240:243], 0\n\t"
    "v_exp_f32 v150, v150\n\t"
    "v_exp_f32 v151, v151\n\t"
    "v_exp_f32 v152, v152\n\t"
    "v_cvt_pk_bf16_f32 v145, v146, v147\n\t"
    "v_mfma_f32_32x32x16_bf16 v[128:143], v[188:191], v[244:247], v[128:143]\n\t"
    "v_exp_f32 v153, v153\n\t"
    "v_exp_f32 v154, v154\n\t"
    "v_cvt_pk_bf16_f32 v146, v148, v149\n\t"
    "v_cvt_pk_bf16_f32 v147, v150, v151\n\t"
    "v_cvt_pk_bf16_f32 v148, v152, v153\n\t"
    "s_waitcnt vmcnt(9)\n\t"
    "v_mfma_f32_32x32x16_bf16 v[32:47], v[216:219], v[204:207], v[32:47]\n\t"
    "v_exp_f32 v155, v155\n\t"
    "v_exp_f32 v156, v156\n\t"
    "v_exp_f32 v157, v157\n\t"
    "v_cvt_pk_bf16_f32 v149, v154, v155\n\t"
    "v_mfma_f32_32x32x16_bf16 v[48:63], v[220:223], v[204:207], v[48:63]\n\t"
    "buffer_load_dwordx4 v[204:207], v252, %[rw], s57 offen offset:1024 sc1\n\t"
    "v_exp_f32 v158, v158\n\t"
    "v_exp_f32 v159, v159\n\t"
    "v_cvt_pk_bf16_f32 v150, v156, v157\n\t"
    "v_cvt_pk_bf16_f32 v151, v158, v159\n\t"
    "v_mfma_f32_32x32x16_bf16 v[80:95], v[192:195], v[144:147], v[80:95]\n\t"
    "ds_read_b64_tr_b16 v[216:217], v249\n\t"
    "ds_read_b64_tr_b16 v[218:219], v249 offset:1024\n\t"
    "ds_read_b64_tr_b16 v[220:221], v250\n\t"
    "ds_read_b64_tr_b16 v[222:223], v250 offset:1024\n\t"
    "v_exp_f32 v128, v128\n\t"
    "v_exp_f32 v129, v129\n\t"
    "v_exp_f32 v130, v130\n\t"
    "v_mfma_f32_32x32x16_bf16 v[80:95], v[196:199], v[148:151], v[80:95]\n\t"
    "v_exp_f32 v131, v131\n\t"
    "v_exp_f32 v132, v132\n\t"
    "v_exp_f32 v133, v133\n\t"
    "v_cvt_pk_bf16_f32 v128, v128, v129\n\t"
    "v_mfma_f32_32x32x16_bf16 v[144:159], v[184:187], v[160:163], 0\n\t"
    "v_exp_f32 v134, v134\n\t"
    "v_exp_f32 v135, v135\n\t"
    "v_exp_f32 v136, v136\n\t"
    "v_cvt_pk_bf16_f32 v129, v130, v131\n\t"
    "v_mfma_f32_32x32x16_bf16 v[144:159], v[188:191], v[164:167], v[144:159]\n\t"
    "v_exp_f32 v137, v137\n\t"
    "v_exp_f32 v138, v138\n\t"
    "v_cvt_pk_bf16_f32 v130, v132, v133\n\t"
    "v_cvt_pk_bf16_f32 v131, v134, v135\n\t"
    "v_cvt_pk_bf16_f32 v132, v136, v137\n\t"
    "s_waitcnt vmcnt(7) lgkmcnt(0)\n\t"
    "v_mfma_f32_32x32x16_bf16 v[0:15], v[216:219], v[208:211], v[0:15]\n\t"
    "v_exp_f32 v139, v139\n\t"
    "v_exp_f32 v140, v140\n\t"
    "v_exp_f32 v141, v141\n\t"
    "v_cvt_pk_bf16_f32 v133, v138, v139\n\t"
    "v_mfma_f32_32x32x16_bf16 v[16:31], v[220:223], v[208:211], v[16:31]\n\t"
    "buffer_load_dwordx4 v[208:211], v252, %[rw], s57 offen offset:2048 sc1\n\t"
    "v_exp_f32 v142, v142\n\t"
    "v_exp_f32 v143, v143\n\t"
    "v_cvt_pk_bf16_f32 v134, v140, v141\n\t"
    "v_cvt_pk_bf16_f32 v135, v142, v143\n\t"
    "v_add_u32 v183, s70, v252\n\t"
    "s_mov_b32 s94, 0\n\t"
    "s_mov_b32 s95, -1\n\t"
    "v_cvt_pk_bf16_f32 v184, v76, v76\n\t"
    "v_cvt_pk_bf16_f32 v185, v66, v67\n\t"
    "v_lshlrev_b32 v184, 16, v184\n\t"
    "v_cvt_pk_bf16_f32 v186, v68, v69\n\t"
    "v_sub_f32 v184, v76, v184\n\t"
    "v_cvt_pk_bf16_f32 v187, v70, v71\n\t"
    "v_cndmask_b32 v77, v77, v184, s[94:95]\n\t"
    "v_cvt_pk_bf16_f32 v184, v64, v65\n\t"
    "v_cvt_pk_bf16_f32 v188, v72, v73\n\t"
    "v_cvt_pk_bf16_f32 v189, v74, v75\n\t"
    "v_cvt_pk_bf16_f32 v190, v76, v77\n\t"
    "v_cvt_pk_bf16_f32 v191, v78, v79\n\t"
    "ds_write_b128 v183, v[184:187]\n\t"
    "ds_write_b128 v183, v[188:191] offset:1024\n\t"
    "v_mfma_f32_32x32x16_bf16 v[96:111], v[192:195], v[128:131], v[96:111]\n\t"
    "v_exp_f32 v144, v144\n\t"
    "v_exp_f32 v145, v145\n\t"
    "v_exp_f32 v146, v146\n\t"
    "v_exp_f32 v147, v147\n\t"
    "v_mfma_f32_32x32x16_bf16 v[96:111], v[196:199], v[132:135], v[96:111]\n\t"
    "v_exp_f32 v148, v148\n\t"
    "v_exp_f32 v149, v149\n\t"
    "v_exp_f32 v150, v150\n\t"
    "v_exp_f32 v151, v151\n\t"
    "v_cvt_pk_bf16_f32 v144, v144, v145\n\t"
    "v_cvt_pk_bf16_f32 v145, v146, v147\n\t"
    "s_waitcnt vmcnt(7)\n\t"
    "v_mfma_f32_32x32x16_bf16 v[32:47], v[216:219], v[212:215], v[32:47]\n\t"
    "v_exp_f32 v152, v152\n\t"
    "v_exp_f32 v153, v153\n\t"
    "v_exp_f32 v154, v154\n\t"
    "v_cvt_pk_bf16_f32 v146, v148, v149\n\t"
    "v_cvt_pk_bf16_f32 v147, v150, v151\n\t"
    "v_mfma_f32_32x32x16_bf16 v[48:63], v[220:223], v[212:215], v[48:63]\n\t"
    "buffer_load_dwordx4 v[212:215], v252, %[rw], s57 offen offset:3072 sc1\n\t"
    "ds_read_b64_tr_b16 v[216:217], v249 offset:16384\n\t"
    "ds_read_b64_tr_b16 v[218:219], v249 offset:17408\n\t"
    "ds_read_b64_tr_b16 v[220:221], v250 offset:16384\n\t"
    "ds_read_b64_tr_b16 v[222:223], v250 offset:17408\n\t"
    "s_waitcnt vmcnt(6)\n\t"
    "ds_read_b64_tr_b16 v[172:173], v249 offset:32768\n\t"
    "ds_read_b64_tr_b16 v[174:175], v249 offset:33792\n\t"
    "ds_read_b64_tr_b16 v[176:177], v250 offset:32768\n\t"
    "ds_read_b64_tr_b16 v[178:179], v250 offset:33792\n\t"
    "v_exp_f32 v155, v155\n\t"
    "v_exp_f32 v156, v156\n\t"
    "v_exp_f32 v157, v157\n\t"
    "v_mfma_f32_32x32x16_bf16 v[112:127], v[192:195], v[144:147], v[112:127]\n\t"
    "v_exp_f32 v158, v158\n\t"
    "v_exp_f32 v159, v159\n\t"
    "v_cvt_pk_bf16_f32 v148, v152, v153\n\t"
    "v_cvt_pk_bf16_f32 v149, v154, v155\n\t"
    "v_cvt_pk_bf16_f32 v150, v156, v157\n\t"
    "v_cvt_pk_bf16_f32 v151, v158, v159\n\t"
    "s_nop 1\n\t"
    "v_mfma_f32_32x32x16_bf16 v[112:127], v[196:199], v[148:151], v[112:127]\n\t"
    "v_cvt_pk_bf16_f32 v184, v92, v92\n\t"
    "v_cvt_pk_bf16_f32 v185, v82, v83\n\t"
    "v_lshlrev_b32 v184, 16, v184\n\t"
    "v_cvt_pk_bf16_f32 v186, v84, v85\n\t"
    "v_sub_f32 v184, v92, v184\n\t"
    "v_cvt_pk_bf16_f32 v187, v86, v87\n\t"
    "v_cndmask_b32 v93, v93, v184, s[94:95]\n\t"
    "v_cvt_pk_bf16_f32 v184, v80, v81\n\t"
    "v_cvt_pk_bf16_f32 v188, v88, v89\n\t"
    "v_cvt_pk_bf16_f32 v189, v90, v91\n\t"
    "v_cvt_pk_bf16_f32 v190, v92, v93\n\t"
    "v_cvt_pk_bf16_f32 v191, v94, v95\n\t"
    "ds_write_b128 v183, v[184:187] offset:16384\n\t"
    "ds_write_b128 v183, v[188:191] offset:17408\n\t"
    "v_cvt_pk_bf16_f32 v184, v108, v108\n\t"
    "v_cvt_pk_bf16_f32 v185, v98, v99\n\t"
    "v_lshlrev_b32 v184, 16, v184\n\t"
    "v_cvt_pk_bf16_f32 v186, v100, v101\n\t"
    "v_sub_f32 v184, v108, v184\n\t"
    "v_cvt_pk_bf16_f32 v187, v102, v103\n\t"
    "v_cndmask_b32 v109, v109, v184, s[94:95]\n\t"
    "v_cvt_pk_bf16_f32 v184, v96, v97\n\t"
    "v_cvt_pk_bf16_f32 v188, v104, v105\n\t"
    "v_cvt_pk_bf16_f32 v189, v106, v107\n\t"
    "v_cvt_pk_bf16_f32 v190, v108, v109\n\t"
    "v_cvt_pk_bf16_f32 v191, v110, v111\n\t"
    "ds_write_b128 v183, v[184:187] offset:32768\n\t"
    "ds_write_b128 v183, v[188:191] offset:33792\n\t"
    "v_cvt_pk_bf16_f32 v184, v124, v124\n\t"
    "v_cvt_pk_bf16_f32 v185, v114, v115\n\t"
    "v_lshlrev_b32 v184, 16, v184\n\t"
    "v_cvt_pk_bf16_f32 v186, v116, v117\n\t"
    "v_sub_f32 v184, v124, v184\n\t"
    "v_cvt_pk_bf16_f32 v187, v118, v119\n\t"
    "v_cndmask_b32 v125, v125, v184, s[94:95]\n\t"
    "v_cvt_pk_bf16_f32 v184, v112, v113\n\t"
    "v_cvt_pk_bf16_f32 v188, v120, v121\n\t"
    "v_cvt_pk_bf16_f32 v189, v122, v123\n\t"
    "v_cvt_pk_bf16_f32 v190, v124, v125\n\t"
    "v_cvt_pk_bf16_f32 v191, v126, v127\n\t"
    "ds_write_b128 v183, v[184:187] offset:49152\n\t"
    "ds_write_b128 v183, v[188:191] offset:50176\n\t"
    "s_setprio 0\n\t"
    "s_waitcnt vmcnt(3)\n\t"
    "s_waitcnt lgkmcnt(0)\n\t"
    "s_barrier\n\t"
    "ds_read_b128 v[132:135], v168 offset:1024\n\t"
    "ds_read_b128 v[128:131], v168\n\t"
    "ds_read_b128 v[140:143], v168 offset:5120\n\t"
    "ds_read_b128 v[136:139], v168 offset:4096\n\t"
    "ds_read_b128 v[148:151], v168 offset:9216\n\t"
    "ds_read_b128 v[144:147], v168 offset:8192\n\t"
    "ds_read_b128 v[156:159], v168 offset:13312\n\t"
    "ds_read_b128 v[152:155], v168 offset:12288\n\t"
    "s_cmp_eq_u32 %[wv], 7\n\t"
    "s_cselect_b64 exec, 1, 0\n\t"
    "s_cbranch_execz Lnoh_%=\n\t"
    "buffer_store_dwordx2 v[170:171], off, %[rw], s67 sc1\n\t"
    "Lnoh_%=:\n\t"
    "s_mov_b64 exec, -1\n\t"
    "s_mov_b32 s82, 0xffff0000\n\t"
    "s_mov_b32 s83, 0xffff0000\n\t"
    "v_mov_b32 v192, 0x3f803f80\n\t"
    "v_mov_b32 v188, 0\n\t"
    "v_mov_b32 v189, 0\n\t"
    "v_mov_b32 v191, 0\n\t"
    "v_cndmask_b32 v190, 0, v192, s[82:83]\n\t"
    "s_mov_b32 s92, 0x300003\n\t"
    "s_mov_b32 s93, 0x300003\n\t"
    "v_cndmask_b32 v184, 0, v169, s[92:93]\n\t"
    "s_mov_b32 s82, 0xc0000c\n\t"
    "s_mov_b32 s83, 0xc0000c\n\t"
    "v_cndmask_b32 v185, 0, v169, s[82:83]\n\t"
    "s_mov_b32 s92, 0x30000300\n\t"
    "s_mov_b32 s93, 0x30000300\n\t"
    "v_cndmask_b32 v186, 0, v169, s[92:93]\n\t"
    "s_mov_b32 s82, 0xc0000c00\n\t"
    "s_mov_b32 s83, 0xc0000c00\n\t"
    "v_cndmask_b32 v187, 0, v169, s[82:83]\n\t"
    "s_waitcnt lgkmcnt(7)\n\t"
    "v_mfma_f32_16x16x32_bf16 v[160:163], v[188:191], v[132:135], 0\n\t"
    "v_mfma_f32_16x16x32_bf16 v[196:199], v[184:187], v[132:135], 0\n\t"
    "s_waitcnt lgkmcnt(6)\n\t"
    "v_mfma_f32_16x16x32_bf16 v[192:195], v[184:187], v[128:131], 0\n\t"
    "s_waitcnt lgkmcnt(5)\n\t"
    "v_mfma_f32_16x16x32_bf16 v[160:163], v[188:191], v[140:143], v[160:163]\n\t"
    "v_mfma_f32_16x16x32_bf16 v[196:199], v[184:187], v[140:143], v[196:199]\n\t"
    "s_waitcnt lgkmcnt(4)\n\t"
    "v_mfma_f32_16x16x32_bf16 v[192:195], v[184:187], v[136:139], v[192:195]\n\t"
    "s_waitcnt lgkmcnt(3)\n\t"
    "v_mfma_f32_16x16x32_bf16 v[160:163], v[188:191], v[148:151], v[160:163]\n\t"
    "v_mfma_f32_16x16x32_bf16 v[196:199], v[184:187], v[148:151], v[196:199]\n\t"
    "s_waitcnt lgkmcnt(2)\n\t"
    "v_mfma_f32_16x16x32_bf16 v[192:195], v[184:187], v[144:147], v[192:195]\n\t"
    "s_waitcnt lgkmcnt(1)\n\t"
    "v_mfma_f32_16x16x32_bf16 v[160:163], v[188:191], v[156:159], v[160:163]\n\t"
    "v_mfma_f32_16x16x32_bf16 v[196:199], v[184:187], v[156:159], v[196:199]\n\t"
    "s_waitcnt lgkmcnt(0)\n\t"
    "v_mfma_f32_16x16x32_bf16 v[192:195], v[184:187], v[152:155], v[192:195]\n\t"
    "v_and_b32 v248, 0x30, v252\n\t"
    "v_mov_b32 v168, 131072\n\t"
    "v_lshlrev_b32 v248, 3, v248\n\t"
    "s_nop 1\n\t"
    "v_rcp_f32 v161, v160\n\t"
    "v_cmp_lt_f32 vcc, s86, v160\n\t"
    "v_cmp_gt_f32 s[82:83], s87, v160\n\t"
    "v_mul_f32 v161, v161, v180\n\t"
    "s_and_b64 vcc, vcc, s[82:83]\n\t"
    "v_mul_f32 v192, v192, v161\n\t"
    "v_mul_f32 v193, v193, v161\n\t"
    "v_mul_f32 v194, v194, v161\n\t"
    "v_mul_f32 v195, v195, v161\n\t"
    "v_mul_f32 v196, v196, v161\n\t"
    "v_mul_f32 v197, v197, v161\n\t"
    "v_mul_f32 v198, v198, v161\n\t"
    "v_mul_f32 v199, v199, v161\n\t"
    "v_add_f32 v162, |v196|, |v197|\n\t"
    "v_add_f32 v163, |v198|, |v199|\n\t"
    "v_add_f32 v162, v162, |v192|\n\t"
    "v_add_f32 v163, v163, |v194|\n\t"
    "v_add_f32 v162, v162, |v193|\n\t"
    "v_add_f32 v163, v163, |v195|\n\t"
    "v_cvt_pk_bf16_f32 v130, v196, v197\n\t"
    "v_add_f32 v162, v162, v163\n\t"
    "v_cvt_pk_bf16_f32 v131, v198, v199\n\t"
    "v_cmp_gt_f32 s[82:83], s88, v162\n\t"
    "v_cvt_pk_bf16_f32 v128, v192, v193\n\t"
    "v_cvt_pk_bf16_f32 v129, v194, v195\n\t"
    "s_and_b64 vcc, vcc, s[82:83]\n\t"
    "v_cndmask_b32 v130, v130, 0, s[96:97]\n\t"
    "v_cndmask_b32 v131, v131, 0, s[96:97]\n\t"
    "s_cmp_eq_u64 vcc, -1\n\t"
    "s_cselect_b32 %[ok], 1, 0\n\t"
    "s_cbranch_scc0 Lbad_%=\n\t"
    "buffer_store_dwordx4 v[128:131], v182, %[rw], s66 offen sc1\n\t"
    "s_branch Lcomb_%=\n\t"
    "Lbad_%=:\n\t"
    "ds_write_b32 v168, v168\n\t"
    "s_waitcnt vmcnt(0)\n\t"
    "Lcomb_%=:\n\t"
    "s_lshr_b32 s89, %[wv], 1\n\t"
    "s_mul_i32 s81, s89, 14336\n\t"
    "s_sub_u32 s81, s81, s70\n\t"
    "v_and_b32 v187, 0x200, v252\n\t"
    "v_add_u32 v188, s81, v249\n\t"
    "v_add_u32 v189, s81, v250\n\t"
    "v_sub_u32 v190, v188, v187\n\t"
    "v_sub_u32 v191, v189, v187\n\t"
    "v_sub_f32 v181, 1.0, v181\n\t"
    "v_sub_f32 v251, 1.0, v251\n\t"
    "v_mfma_f32_32x32x16_bf16 v[0:15], v[216:219], v[200:203], v[0:15]\n\t"
    "ds_read_b64_tr_b16 v[64:65], v188\n\t"
    "ds_read_b64_tr_b16 v[66:67], v188 offset:2048\n\t"
    "ds_read_b64_tr_b16 v[68:69], v189\n\t"
    "ds_read_b64_tr_b16 v[70:71], v189 offset:2048\n\t"
    "v_mfma_f32_32x32x16_bf16 v[16:31], v[220:223], v[200:203], v[16:31]\n\t"
    "ds_read_b64_tr_b16 v[72:73], v188 offset:1024\n\t"
    "ds_read_b64_tr_b16 v[74:75], v190 offset:3072\n\t"
    "ds_read_b64_tr_b16 v[76:77], v189 offset:1024\n\t"
    "ds_read_b64_tr_b16 v[78:79], v191 offset:3072\n\t"
    "s_waitcnt vmcnt(2)\n\t"
    "v_mfma_f32_32x32x16_bf16 v[0:15], v[172:175], v[208:211], v[0:15]\n\t"
    "ds_read_b64_tr_b16 v[80:81], v188 offset:3584\n\t"
    "ds_read_b64_tr_b16 v[82:83], v188 offset:5632\n\t"
    "ds_read_b64_tr_b16 v[84:85], v189 offset:3584\n\t"
    "ds_read_b64_tr_b16 v[86:87], v189 offset:5632\n\t"
    "v_mfma_f32_32x32x16_bf16 v[16:31], v[176:179], v[208:211], v[16:31]\n\t"
    "ds_read_b64_tr_b16 v[88:89], v188 offset:4608\n\t"
    "ds_read_b64_tr_b16 v[90:91], v190 offset:6656\n\t"
    "ds_read_b64_tr_b16 v[92:93], v189 offset:4608\n\t"
    "ds_read_b64_tr_b16 v[94:95], v191 offset:6656\n\t"
    "v_mfma_f32_32x32x16_bf16 v[32:47], v[216:219], v[204:207], v[32:47]\n\t"
    "ds_read_b64_tr_b16 v[96:97], v188 offset:7168\n\t"
    "ds_read_b64_tr_b16 v[98:99], v188 offset:9216\n\t"
    "ds_read_b64_tr_b16 v[100:101], v189 offset:7168\n\t"
    "ds_read_b64_tr_b16 v[102:103], v189 offset:9216\n\t"
    "v_mfma_f32_32x32x16_bf16 v[48:63], v[220:223], v[204:207], v[48:63]\n\t"
    "ds_read_b64_tr_b16 v[104:105], v188 offset:8192\n\t"
    "ds_read_b64_tr_b16 v[106:107], v190 offset:10240\n\t"
    "ds_read_b64_tr_b16 v[108:109], v189 offset:8192\n\t"
    "ds_read_b64_tr_b16 v[110:111], v191 offset:10240\n\t"
    "s_waitcnt vmcnt(1)\n\t"
    "v_mfma_f32_32x32x16_bf16 v[32:47], v[172:175], v[212:215], v[32:47]\n\t"
    "ds_read_b64_tr_b16 v[112:113], v188 offset:10752\n\t"
    "ds_read_b64_tr_b16 v[114:115], v188 offset:12800\n\t"
    "ds_read_b64_tr_b16 v[116:117], v189 offset:10752\n\t"
    "ds_read_b64_tr_b16 v[118:119], v189 offset:12800\n\t"
    "v_mfma_f32_32x32x16_bf16 v[48:63], v[176:179], v[212:215], v[48:63]\n\t"
    "buffer_load_dwordx2 v[244:245], v248, %[rw], s68 offen sc1\n\t"
    "ds_read_b64_tr_b16 v[120:121], v188 offset:11776\n\t"
    "ds_read_b64_tr_b16 v[122:123], v190 offset:13824\n\t"
    "ds_read_b64_tr_b16 v[124:125], v189 offset:11776\n\t"
    "ds_read_b64_tr_b16 v[126:127], v191 offset:13824\n\t"
    "v_mul_f32 v0, v0, v181\n\t"
    "v_mul_f32 v1, v1, v181\n\t"
    "v_mul_f32 v2, v2, v181\n\t"
    "v_mul_f32 v3, v3, v181\n\t"
    "v_mul_f32 v4, v4, v181\n\t"
    "v_mul_f32 v5, v5, v181\n\t"
    "v_mul_f32 v6, v6, v181\n\t"
    "v_mul_f32 v7, v7, v181\n\t"
    "v_mul_f32 v8, v8, v181\n\t"
    "v_mul_f32 v9, v9, v181\n\t"
    "v_mul_f32 v10, v10, v181\n\t"
    "v_mul_f32 v11, v11, v181\n\t"
    "v_mul_f32 v12, v12, v181\n\t"
    "v_mul_f32 v13, v13, v181\n\t"
    "v_mul_f32 v14, v14, v181\n\t"
    "v_mul_f32 v15, v15, v181\n\t"
    "v_mul_f32 v16, v16, v181\n\t"
    "v_mul_f32 v17, v17, v181\n\t"
    "v_mul_f32 v18, v18, v181\n\t"
    "v_mul_f32 v19, v19, v181\n\t"
    "v_mul_f32 v20, v20, v181\n\t"
    "v_mul_f32 v21, v21, v181\n\t"
    "v_mul_f32 v22, v22, v181\n\t"
    "v_mul_f32 v23, v23, v181\n\t"
    "v_mul_f32 v24, v24, v181\n\t"
    "v_mul_f32 v25, v25, v181\n\t"
    "v_mul_f32 v26, v26, v181\n\t"
    "v_mul_f32 v27, v27, v181\n\t"
    "v_mul_f32 v28, v28, v181\n\t"
    "v_mul_f32 v29, v29, v181\n\t"
    "v_mul_f32 v30, v30, v181\n\t"
    "v_mul_f32 v31, v31, v181\n\t"
    "s_waitcnt lgkmcnt(0)\n\t"
    "s_barrier\n\t"
    "ds_read_b32 v168, v168\n\t"
    "s_waitcnt lgkmcnt(0)\n\t"
    "v_readfirstlane_b32 %[redo], v168\n\t"
    "s_nop 0\n\t"
    "s_lshr_b32 s80, %[bid], 5\n\t"
    "s_cmp_eq_u32 s80, 7\n\t"
    "s_cselect_b32 s80, 1, %[redo]\n\t"
    "s_cmp_eq_u32 s80, 0\n\t"
    "s_cbranch_scc1 Lout_%=\n\t"
    "s_waitcnt vmcnt(0)\n\t"
    "Lout_%=:\n\t"
    "s_nop 1\n\t"
      : "=&{v[0:15]}"(d00), "=&{v[16:31]}"(d01), "=&{v[32:47]}"(d10), "=&{v[48:63]}"(d11),
        "=&{v[64:79]}"(wo0), "=&{v[80:95]}"(wo1), "=&{v[96:111]}"(wo2), "=&{v[112:127]}"(wo3),
        "+{v216}"(vx), "+{v217}"(v2), "+{v218}"(v3), "+{v219}"(v4), "+{v220}"(A0), "+{v221}"(A1),
        "+{v232}"(xb0), "+{v233}"(xb1), "+{v234}"(xb2), "+{v235}"(xb3), "=&{v180}"(gq), "+{v244}"(h0), "=&{v245}"(h1),
        "+{v252}"(l16), "+{v248}"(hv), "+{v249}"(W0), "=&{v251}"(gr), [ok] "=&s"(okw), [redo] "=&s"(redo),
        "=&{s68}"(hc), "=&{s69}"(ob), "=&{s70}"(mring), "=&{s[76:77]}"(op), "=&{s[84:85]}"(ep)
      : [rw] "s"(rw), [rd] "s"(rd), [ro] "s"(ro), [rx] "s"(rx), [rq] "s"(rq), [r3] "s"(r3), [r4] "s"(r4), [gp] "s"(gw), [out] "s"(out), [sx] "s"(sx), [s2] "s"(s2),
        [s3] "s"(s3), [st34] "s"(st34), [rot] "s"(rot), [ldw] "s"(ldw), [ty] "s"(ty),
        [wv] "s"(w), [bid] "s"(bid), [rb] "s"(rb), [cs] "s"(cs)
      : "memory", "vcc", "scc",
        "v128", "v129", "v130", "v131", "v132", "v133", "v134", "v135", "v136", "v137", "v138", "v139", "v140", "v141", "v142", "v143", "v144", "v145",
        "v146", "v147", "v148", "v149", "v150", "v151", "v152", "v153", "v154", "v155", "v156", "v157", "v158", "v159", "v160", "v161", "v162", "v163",
        "v164", "v165", "v166", "v167", "v168", "v169", "v170", "v171", "v172", "v173", "v174", "v175", "v176", "v177", "v178", "v179", "v181", "v182",
        "v183", "v184", "v185", "v186", "v187", "v188", "v189", "v190", "v191", "v192", "v193", "v194", "v195", "v196", "v197", "v198", "v199", "v200",
        "v201", "v202", "v203", "v204", "v205", "v206", "v207", "v208", "v209", "v210", "v211", "v212", "v213", "v214", "v215", "v222", "v223", "v224",
        "v225", "v226", "v227", "v228", "v229", "v230", "v231", "v236", "v237", "v238", "v239", "v240", "v241", "v242", "v243", "v246", "v247", "v250",
        "v254", "v255", "s46", "s47", "s48", "s50", "s51", "s52", "s53", "s54", "s55", "s56", "s57", "s58", "s59", "s60", "s61", "s62", "s63", "s64", "s65",
        "s66", "s67", "s71", "s72", "s73", "s74", "s75", "s78", "s79", "s80", "s81", "s82", "s83", "s86", "s87", "s88", "s89", "s90", "s91", "s92", "s93",
        "s94", "s95", "s96", "s97"
    ); }
  if (__builtin_expect(redo != 0, 0)) {
    const int lanee = lane_now(), hhe = lanee >> 5, blke = (lanee >> 4) & 1;
    unsigned b2 = bid; asm volatile("" : "+s"(b2));
    const unsigned head = 2 * (b2 & 7) + (b2 >> 7), qb = (b2 >> 3) & 15, kvb = WS_KV + (head * 64 + 8 * w) * 4096;
    const unsigned os = WS_O + ((qb * 4 + (w >> 1)) * 16 + head) * 2048 + 256 * (w & 1);
    const unsigned le = lanee * 16; const bool pad = (head & 1) ? !hhe : hhe;
#pragma unroll 1
    for (int qt = 0; qt < 4; ++qt) {
      u32x4 y0 = gld(rw, le, WS_Q + (head * 64 + qb * 4 + qt) * 2048), y1 = gld(rw, le, WS_Q + (head * 64 + qb * 4 + qt) * 2048 + 1024);
      if (pad) { if (head & 1) { y0[0] = 0; y0[1] = 0; } else { y1[2] = 0; y1[3] = 0; } }
      float m = -__builtin_inff();
#pragma unroll 1
      for (int kt = 0; kt < 8; ++kt) {
        f32x16 s = MFMA(asbf(gld(rw, le, kvb + kt * 4096)), asbf(y0), f32x16{}); s = MFMA(asbf(gld(rw, le, kvb + kt * 4096 + 1024)), asbf(y1), s);
        m = __builtin_fmaxf(m, max16(s));
      }
      m = halfmax(m);
      *(LAS float*)LP(LDS_QX + w * 256 + lanee * 4) = m;
      BAR();
#pragma unroll
      for (int sw = 0; sw < 8; ++sw) m = __builtin_fmaxf(m, *(LAS float*)LP(LDS_QX + sw * 256 + lanee * 4));
      BAR();
      f32x16 o = {};
#pragma unroll 1
      for (int kt = 0; kt < 8; ++kt) {
        f32x16 s = MFMA(asbf(gld(rw, le, kvb + kt * 4096)), asbf(y0), f32x16{}); s = MFMA(asbf(gld(rw, le, kvb + kt * 4096 + 1024)), asbf(y1), s);
#pragma unroll
        for (int i = 0; i < 16; ++i) s[i] = __builtin_amdgcn_exp2f(s[i] - m);
        o = MFMA(asbf(gld(rw, le, kvb + kt * 4096 + 2048)), asbf(pack8(s, 0)), o); o = MFMA(asbf(gld(rw, le, kvb + kt * 4096 + 3072)), asbf(pack8(s, 1)), o);
      }
      const float lo = o[12] - __uint_as_float(cvtpk(o[12], 0.f) << 16);
      o[13] = hhe ? lo : o[13];
      ldsw128(LDS_RING + w * 2048 + le + qt * 16384, 0, pack8(o, 0)); ldsw128(LDS_RING + w * 2048 + le + qt * 16384, 1024, pack8(o, 1));
    }
    BAR();
    const int rho = lanee & 15, kg = lanee >> 4, dw = 2 * (rho >> 3) + ((rho >> 1) & 1);
    const unsigned one = ((rho >> 2) & 1) == (kg & 1) ? (rho & 1 ? 0x3F800000u : 0x3F80u) : 0u;
    const u32x4 sel = {dw == 0 ? one : 0u, dw == 1 ? one : 0u, dw == 2 ? one : 0u, dw == 3 ? one : 0u}, sell = {0u, 0u, (kg & 1) ? 0x3F803F80u : 0u, 0u};
    const unsigned PR = LDS_RING + (w >> 1) * 16384 + hhe * 2048 + (16 * (w & 1) + rho + 32 * blke) * 16;
    f32x4 x0 = {}, x1 = {}, xl = {};
#pragma unroll
    for (int sp = 0; sp < 4; ++sp) {
      const bf16x8 f0 = ldsr128(PR, sp * 4096), f1 = ldsr128(PR, sp * 4096 + 1024);
      x0 = MFMA16(asbf(sel), f0, x0); x1 = MFMA16(asbf(sel), f1, x1); xl = MFMA16(asbf(sell), f1, xl);
    }
    const float sc = gq * __builtin_amdgcn_rcpf(xl[0]);
    u32x4 ov = {cvtpk(x0[0] * sc, x0[1] * sc), cvtpk(x0[2] * sc, x0[3] * sc), cvtpk(x1[0] * sc, x1[1] * sc), cvtpk(x1[2] * sc, x1[3] * sc)};
    if (lanee >= 48) { ov[2] = 0; ov[3] = 0; }
#pragma unroll
    for (int i = 0; i < 4; ++i) ov[i] = ov[i] == SENT ? SENT - 1 : ov[i];
    if (!okw) gst(rw, ov, kg * 512 + rho * 16, os);
    BAR();
  }
  if (bid >= 224) {
    if (bid == 255 && w == 0) {
      const u32x2 gv = {(unsigned)ep, (unsigned)(ep >> 32)};
      const unsigned long long t0 = __builtin_amdgcn_s_memrealtime();
      for (;;) {
        bool ok = true;
#pragma unroll
        for (int i = 0; i < 4; ++i) { const u32x2 f = __builtin_amdgcn_raw_buffer_load_b64(rw, WS_FLAG + i * 8192 + l16 * 8, 0, AUX_DEV); ok = ok && f[0] == gv[0] && f[1] == gv[1]; }
        if (__all(ok) || __builtin_amdgcn_s_memrealtime() - t0 > SPIN_TICKS) break;
        RELOAD();
      }
      if (l16 == 0) gst8(rw, gv, WS_GEN, 0);
    }
    return;
  }

  asm volatile(
    "s_add_u32 s80, s69, 4096\n\t"
    "s_mov_b32 s81, 0\n\t"
    "s_mov_b32 s89, 0\n\t"
    "s_mov_b32 s90, 0\n\t"
    "Lnp_%=:\n\t"
    "v_and_b32 v224, 0x1f0, v252\n\t"
    "v_lshrrev_b32 v225, 5, v252\n\t"
    "v_and_b32 v226, 0x60, v252\n\t"
    "s_lshl_b32 s87, %[wv], 13\n\t"
    "s_xor_b32 s87, s87, 0x2000\n\t"
    "v_xor_b32 v225, v225, v252\n\t"
    "v_and_or_b32 v225, v225, 16, v226\n\t"
    "v_lshl_or_b32 v225, v224, 4, v225\n\t"
    "v_add_u32 v232, s87, v225\n\t"
    "v_xor_b32 v233, 32, v232\n\t"
    "v_xor_b32 v234, 64, v232\n\t"
    "v_xor_b32 v235, 96, v232\n\t"
    "ds_write_b128 v232, v[32:35]\n\t"
    "v_xor_b32 v228, 73728, v232\n\t"
    "v_xor_b32 v229, 73760, v232\n\t"
    "v_xor_b32 v230, 73792, v232\n\t"
    "v_xor_b32 v231, 73824, v232\n\t"
    "ds_write_b128 v233, v[36:39]\n\t"
    "v_lshrrev_b32 v226, 7, v252\n\t"
    "v_lshlrev_b32 v224, 1, v252\n\t"
    "v_and_b32 v224, 0xe0, v224\n\t"
    "v_lshlrev_b32 v225, 4, v226\n\t"
    "s_cmp_eq_u32 s90, -1\n\t"
    "s_cbranch_scc1 Lne_%=\n\t"
    "s_waitcnt vmcnt(0)\n\t"
    "s_mov_b32 s90, s89\n\t"
    "v_cmp_eq_u64 vcc, s[84:85], v[244:245]\n\t"
    "s_cmp_gt_u32 s81, 0x60000\n\t"
    "s_cselect_b32 s86, -1, vcc_lo\n\t"
    "s_andn2_b32 s86, s86, s89\n\t"
    "s_or_b32 s89, s89, s86\n\t"
    "s_bitcmp1_b32 s86, 0\n\t"
    "s_cbranch_scc0 Leva0_%=\n\t"
    "buffer_load_dwordx4 v[192:195], v252, %[rw], s69 offen sc1\n\t"
    "buffer_load_dwordx4 v[196:199], v252, %[rw], s69 offen offset:1024 sc1\n\t"
    "Leva0_%=:\n\t"
    "s_bitcmp1_b32 s86, 1\n\t"
    "s_cbranch_scc0 Leva1_%=\n\t"
    "buffer_load_dwordx4 v[200:203], v252, %[rw], s69 offen offset:2048 sc1\n\t"
    "buffer_load_dwordx4 v[204:207], v252, %[rw], s69 offen offset:3072 sc1\n\t"
    "Leva1_%=:\n\t"
    "s_bitcmp1_b32 s86, 2\n\t"
    "s_cbranch_scc0 Leva2_%=\n\t"
    "buffer_load_dwordx4 v[208:211], v252, %[rw], s80 offen sc1\n\t"
    "buffer_load_dwordx4 v[212:215], v252, %[rw], s80 offen offset:1024 sc1\n\t"
    "Leva2_%=:\n\t"
    "s_bitcmp1_b32 s86, 3\n\t"
    "s_cbranch_scc0 Leva3_%=\n\t"
    "buffer_load_dwordx4 v[216:219], v252, %[rw], s80 offen offset:2048 sc1\n\t"
    "buffer_load_dwordx4 v[220:223], v252, %[rw], s80 offen offset:3072 sc1\n\t"
    "Leva3_%=:\n\t"
    "s_and_b32 s86, s86, 15\n\t"
    "s_cmp_eq_u32 s86, 15\n\t"
    "s_cselect_b32 s90, -1, s90\n\t"
    "Lne_%=:\n\t"
    "ds_write_b128 v234, v[40:43]\n\t"
    "v_xor_b32 v224, v224, v225\n\t"
    "v_lshl_or_b32 v224, v226, 8, v224\n\t"
    "v_add_u32 v224, s70, v224\n\t"
    "v_xor_b32 v225, 16, v224\n\t"
    "ds_write_b128 v235, v[44:47]\n\t"
    "v_mul_u32_u24 v226, 896, v226\n\t"
    "v_and_b32 v237, 0x70, v252\n\t"
    "v_add_u32 v226, v226, v237\n\t"
    "ds_write_b128 v232, v[48:51] offset:128\n\t"
    "v_xor_b32 v237, 0x10000, v224\n\t"
    "v_xor_b32 v238, 0x10000, v225\n\t"
    "ds_write_b128 v233, v[52:55] offset:128\n\t"
    "ds_write_b128 v234, v[56:59] offset:128\n\t"
    "ds_write_b128 v235, v[60:63] offset:128\n\t"
    "Lt0_%=:\n\t"
    "s_bitcmp1_b32 s90, 0\n\t"
    "s_cbranch_scc1 Lchk0_%=\n\t"
    "buffer_load_dwordx2 v[244:245], v248, %[rw], s68 offen sc1\n\t"
    "s_add_u32 s81, s81, 1\n\t"
    "s_waitcnt vmcnt(0)\n\t"
    "s_mov_b32 s90, s89\n\t"
    "v_cmp_eq_u64 vcc, s[84:85], v[244:245]\n\t"
    "s_cmp_gt_u32 s81, 0x60000\n\t"
    "s_cselect_b32 s86, -1, vcc_lo\n\t"
    "s_andn2_b32 s86, s86, s89\n\t"
    "s_or_b32 s89, s89, s86\n\t"
    "s_bitcmp1_b32 s86, 0\n\t"
    "s_cbranch_scc0 Levp00_%=\n\t"
    "buffer_load_dwordx4 v[192:195], v252, %[rw], s69 offen sc1\n\t"
    "buffer_load_dwordx4 v[196:199], v252, %[rw], s69 offen offset:1024 sc1\n\t"
    "Levp00_%=:\n\t"
    "s_bitcmp1_b32 s86, 1\n\t"
    "s_cbranch_scc0 Levp01_%=\n\t"
    "buffer_load_dwordx4 v[200:203], v252, %[rw], s69 offen offset:2048 sc1\n\t"
    "buffer_load_dwordx4 v[204:207], v252, %[rw], s69 offen offset:3072 sc1\n\t"
    "Levp01_%=:\n\t"
    "s_bitcmp1_b32 s86, 2\n\t"
    "s_cbranch_scc0 Levp02_%=\n\t"
    "buffer_load_dwordx4 v[208:211], v252, %[rw], s80 offen sc1\n\t"
    "buffer_load_dwordx4 v[212:215], v252, %[rw], s80 offen offset:1024 sc1\n\t"
    "Levp02_%=:\n\t"
    "s_bitcmp1_b32 s86, 3\n\t"
    "s_cbranch_scc0 Levp03_%=\n\t"
    "buffer_load_dwordx4 v[216:219], v252, %[rw], s80 offen offset:2048 sc1\n\t"
    "buffer_load_dwordx4 v[220:223], v252, %[rw], s80 offen offset:3072 sc1\n\t"
    "Levp03_%=:\n\t"
    "s_branch Lt0_%=\n\t"
    "Lchk0_%=:\n\t"
    "s_waitcnt vmcnt(7)\n\t"
    "v_max3_u32 v236, v192, v193, v194\n\t"
    "v_max_u32 v236, v236, v195\n\t"
    "v_cmp_eq_u32 vcc, -1, v236\n\t"
    "s_cbranch_vccnz Lre0_%=\n\t"
    "Lgo0_%=:\n\t"
    "v_mfma_f32_32x32x16_bf16 v[0:15], v[64:67], v[192:195], v[0:15]\n\t"
    "v_mfma_f32_32x32x16_bf16 v[16:31], v[68:71], v[192:195], v[16:31]\n\t"
    "Lchk1_%=:\n\t"
    "s_waitcnt vmcnt(6)\n\t"
    "v_max3_u32 v236, v196, v197, v198\n\t"
    "v_max_u32 v236, v236, v199\n\t"
    "v_cmp_eq_u32 vcc, -1, v236\n\t"
    "s_cbranch_vccnz Lre1_%=\n\t"
    "Lgo1_%=:\n\t"
    "v_mfma_f32_32x32x16_bf16 v[0:15], v[72:75], v[196:199], v[0:15]\n\t"
    "v_mfma_f32_32x32x16_bf16 v[16:31], v[76:79], v[196:199], v[16:31]\n\t"
    "Lt1_%=:\n\t"
    "s_bitcmp1_b32 s90, 1\n\t"
    "s_cbranch_scc1 Lchk2_%=\n\t"
    "buffer_load_dwordx2 v[244:245], v248, %[rw], s68 offen sc1\n\t"
    "s_add_u32 s81, s81, 1\n\t"
    "s_waitcnt vmcnt(0)\n\t"
    "s_mov_b32 s90, s89\n\t"
    "v_cmp_eq_u64 vcc, s[84:85], v[244:245]\n\t"
    "s_cmp_gt_u32 s81, 0x60000\n\t"
    "s_cselect_b32 s86, -1, vcc_lo\n\t"
    "s_andn2_b32 s86, s86, s89\n\t"
    "s_or_b32 s89, s89, s86\n\t"
    "s_bitcmp1_b32 s86, 0\n\t"
    "s_cbranch_scc0 Levp10_%=\n\t"
    "buffer_load_dwordx4 v[192:195], v252, %[rw], s69 offen sc1\n\t"
    "buffer_load_dwordx4 v[196:199], v252, %[rw], s69 offen offset:1024 sc1\n\t"
    "Levp10_%=:\n\t"
    "s_bitcmp1_b32 s86, 1\n\t"
    "s_cbranch_scc0 Levp11_%=\n\t"
    "buffer_load_dwordx4 v[200:203], v252, %[rw], s69 offen offset:2048 sc1\n\t"
    "buffer_load_dwordx4 v[204:207], v252, %[rw], s69 offen offset:3072 sc1\n\t"
    "Levp11_%=:\n\t"
    "s_bitcmp1_b32 s86, 2\n\t"
    "s_cbranch_scc0 Levp12_%=\n\t"
    "buffer_load_dwordx4 v[208:211], v252, %[rw], s80 offen sc1\n\t"
    "buffer_load_dwordx4 v[212:215], v252, %[rw], s80 offen offset:1024 sc1\n\t"
    "Levp12_%=:\n\t"
    "s_bitcmp1_b32 s86, 3\n\t"
    "s_cbranch_scc0 Levp13_%=\n\t"
    "buffer_load_dwordx4 v[216:219], v252, %[rw], s80 offen offset:2048 sc1\n\t"
    "buffer_load_dwordx4 v[220:223], v252, %[rw], s80 offen offset:3072 sc1\n\t"
    "Levp13_%=:\n\t"
    "s_branch Lt1_%=\n\t"
    "Lchk2_%=:\n\t"
    "s_waitcnt vmcnt(5)\n\t"
    "v_max3_u32 v236, v200, v201, v202\n\t"
    "v_max_u32 v236, v236, v203\n\t"
    "v_cmp_eq_u32 vcc, -1, v236\n\t"
    "s_cbranch_vccnz Lre2_%=\n\t"
    "Lgo2_%=:\n\t"
    "v_mfma_f32_32x32x16_bf16 v[0:15], v[80:83], v[200:203], v[0:15]\n\t"
    "v_mfma_f32_32x32x16_bf16 v[16:31], v[84:87], v[200:203], v[16:31]\n\t"
    "Lchk3_%=:\n\t"
    "s_waitcnt vmcnt(4)\n\t"
    "v_max3_u32 v236, v204, v205, v206\n\t"
    "v_max_u32 v236, v236, v207\n\t"
    "v_cmp_eq_u32 vcc, -1, v236\n\t"
    "s_cbranch_vccnz Lre3_%=\n\t"
    "Lgo3_%=:\n\t"
    "v_mfma_f32_32x32x16_bf16 v[0:15], v[88:91], v[204:207], v[0:15]\n\t"
    "v_mfma_f32_32x32x16_bf16 v[16:31], v[92:95], v[204:207], v[16:31]\n\t"
    "Lt2_%=:\n\t"
    "s_bitcmp1_b32 s90, 2\n\t"
    "s_cbranch_scc1 Lchk4_%=\n\t"
    "buffer_load_dwordx2 v[244:245], v248, %[rw], s68 offen sc1\n\t"
    "s_add_u32 s81, s81, 1\n\t"
    "s_waitcnt vmcnt(0)\n\t"
    "s_mov_b32 s90, s89\n\t"
    "v_cmp_eq_u64 vcc, s[84:85], v[244:245]\n\t"
    "s_cmp_gt_u32 s81, 0x60000\n\t"
    "s_cselect_b32 s86, -1, vcc_lo\n\t"
    "s_andn2_b32 s86, s86, s89\n\t"
    "s_or_b32 s89, s89, s86\n\t"
    "s_bitcmp1_b32 s86, 0\n\t"
    "s_cbranch_scc0 Levp20_%=\n\t"
    "buffer_load_dwordx4 v[192:195], v252, %[rw], s69 offen sc1\n\t"
    "buffer_load_dwordx4 v[196:199], v252, %[rw], s69 offen offset:1024 sc1\n\t"
    "Levp20_%=:\n\t"
    "s_bitcmp1_b32 s86, 1\n\t"
    "s_cbranch_scc0 Levp21_%=\n\t"
    "buffer_load_dwordx4 v[200:203], v252, %[rw], s69 offen offset:2048 sc1\n\t"
    "buffer_load_dwordx4 v[204:207], v252, %[rw], s69 offen offset:3072 sc1\n\t"
    "Levp21_%=:\n\t"
    "s_bitcmp1_b32 s86, 2\n\t"
    "s_cbranch_scc0 Levp22_%=\n\t"
    "buffer_load_dwordx4 v[208:211], v252, %[rw], s80 offen sc1\n\t"
    "buffer_load_dwordx4 v[212:215], v252, %[rw], s80 offen offset:1024 sc1\n\t"
    "Levp22_%=:\n\t"
    "s_bitcmp1_b32 s86, 3\n\t"
    "s_cbranch_scc0 Levp23_%=\n\t"
    "buffer_load_dwordx4 v[216:219], v252, %[rw], s80 offen offset:2048 sc1\n\t"
    "buffer_load_dwordx4 v[220:223], v252, %[rw], s80 offen offset:3072 sc1\n\t"
    "Levp23_%=:\n\t"
    "s_branch Lt2_%=\n\t"
    "Lchk4_%=:\n\t"
    "s_waitcnt vmcnt(3)\n\t"
    "v_max3_u32 v236, v208, v209, v210\n\t"
    "v_max_u32 v236, v236, v211\n\t"
    "v_cmp_eq_u32 vcc, -1, v236\n\t"
    "s_cbranch_vccnz Lre4_%=\n\t"
    "Lgo4_%=:\n\t"
    "v_mfma_f32_32x32x16_bf16 v[0:15], v[96:99], v[208:211], v[0:15]\n\t"
    "v_mfma_f32_32x32x16_bf16 v[16:31], v[100:103], v[208:211], v[16:31]\n\t"
    "Lchk5_%=:\n\t"
    "s_waitcnt vmcnt(2)\n\t"
    "v_max3_u32 v236, v212, v213, v214\n\t"
    "v_max_u32 v236, v236, v215\n\t"
    "v_cmp_eq_u32 vcc, -1, v236\n\t"
    "s_cbranch_vccnz Lre5_%=\n\t"
    "Lgo5_%=:\n\t"
    "v_mfma_f32_32x32x16_bf16 v[0:15], v[104:107], v[212:215], v[0:15]\n\t"
    "v_mfma_f32_32x32x16_bf16 v[16:31], v[108:111], v[212:215], v[16:31]\n\t"
    "Lt3_%=:\n\t"
    "s_bitcmp1_b32 s90, 3\n\t"
    "s_cbranch_scc1 Lchk6_%=\n\t"
    "buffer_load_dwordx2 v[244:245], v248, %[rw], s68 offen sc1\n\t"
    "s_add_u32 s81, s81, 1\n\t"
    "s_waitcnt vmcnt(0)\n\t"
    "s_mov_b32 s90, s89\n\t"
    "v_cmp_eq_u64 vcc, s[84:85], v[244:245]\n\t"
    "s_cmp_gt_u32 s81, 0x60000\n\t"
    "s_cselect_b32 s86, -1, vcc_lo\n\t"
    "s_andn2_b32 s86, s86, s89\n\t"
    "s_or_b32 s89, s89, s86\n\t"
    "s_bitcmp1_b32 s86, 0\n\t"
    "s_cbranch_scc0 Levp30_%=\n\t"
    "buffer_load_dwordx4 v[192:195], v252, %[rw], s69 offen sc1\n\t"
    "buffer_load_dwordx4 v[196:199], v252, %[rw], s69 offen offset:1024 sc1\n\t"
    "Levp30_%=:\n\t"
    "s_bitcmp1_b32 s86, 1\n\t"
    "s_cbranch_scc0 Levp31_%=\n\t"
    "buffer_load_dwordx4 v[200:203], v252, %[rw], s69 offen offset:2048 sc1\n\t"
    "buffer_load_dwordx4 v[204:207], v252, %[rw], s69 offen offset:3072 sc1\n\t"
    "Levp31_%=:\n\t"
    "s_bitcmp1_b32 s86, 2\n\t"
    "s_cbranch_scc0 Levp32_%=\n\t"
    "buffer_load_dwordx4 v[208:211], v252, %[rw], s80 offen sc1\n\t"
    "buffer_load_dwordx4 v[212:215], v252, %[rw], s80 offen offset:1024 sc1\n\t"
    "Levp32_%=:\n\t"
    "s_bitcmp1_b32 s86, 3\n\t"
    "s_cbranch_scc0 Levp33_%=\n\t"
    "buffer_load_dwordx4 v[216:219], v252, %[rw], s80 offen offset:2048 sc1\n\t"
    "buffer_load_dwordx4 v[220:223], v252, %[rw], s80 offen offset:3072 sc1\n\t"
    "Levp33_%=:\n\t"
    "s_branch Lt3_%=\n\t"
    "Lchk6_%=:\n\t"
    "s_waitcnt vmcnt(1)\n\t"
    "v_max3_u32 v236, v216, v217, v218\n\t"
    "v_max_u32 v236, v236, v219\n\t"
    "v_cmp_eq_u32 vcc, -1, v236\n\t"
    "s_cbranch_vccnz Lre6_%=\n\t"
    "Lgo6_%=:\n\t"
    "v_mfma_f32_32x32x16_bf16 v[0:15], v[112:115], v[216:219], v[0:15]\n\t"
    "v_mfma_f32_32x32x16_bf16 v[16:31], v[116:119], v[216:219], v[16:31]\n\t"
    "Lchk7_%=:\n\t"
    "s_waitcnt vmcnt(0)\n\t"
    "v_max3_u32 v236, v220, v221, v222\n\t"
    "v_max_u32 v236, v236, v223\n\t"
    "v_cmp_eq_u32 vcc, -1, v236\n\t"
    "s_cbranch_vccnz Lre7_%=\n\t"
    "Lgo7_%=:\n\t"
    "v_mfma_f32_32x32x16_bf16 v[0:15], v[120:123], v[220:223], v[0:15]\n\t"
    "v_mfma_f32_32x32x16_bf16 v[16:31], v[124:127], v[220:223], v[16:31]\n\t"
    "s_nop 10\n\t"
    "ds_write_b128 v228, v[0:3]\n\t"
    "ds_write_b128 v229, v[4:7]\n\t"
    "ds_write_b128 v230, v[8:11]\n\t"
    "ds_write_b128 v231, v[12:15]\n\t"
    "ds_write_b128 v228, v[16:19] offset:128\n\t"
    "ds_write_b128 v229, v[20:23] offset:128\n\t"
    "ds_write_b128 v230, v[24:27] offset:128\n\t"
    "ds_write_b128 v231, v[28:31] offset:128\n\t"
    "s_waitcnt lgkmcnt(0)\n\t"
    "s_barrier\n\t"
    "ds_read_b128 v[128:131], v237\n\t"
    "ds_read_b128 v[132:135], v238\n\t"
    "ds_read_b128 v[136:139], v237 offset:16384\n\t"
    "ds_read_b128 v[140:143], v238 offset:16384\n\t"
    "ds_read_b128 v[144:147], v237 offset:32768\n\t"
    "ds_read_b128 v[148:151], v238 offset:32768\n\t"
    "ds_read_b128 v[152:155], v237 offset:49152\n\t"
    "ds_read_b128 v[156:159], v238 offset:49152\n\t"
    "ds_read_b128 v[160:163], v224\n\t"
    "ds_read_b128 v[164:167], v225\n\t"
    "ds_read_b128 v[168:171], v224 offset:16384\n\t"
    "ds_read_b128 v[172:175], v225 offset:16384\n\t"
    "ds_read_b128 v[176:179], v224 offset:32768\n\t"
    "ds_read_b128 v[180:183], v225 offset:32768\n\t"
    "ds_read_b128 v[184:187], v224 offset:49152\n\t"
    "ds_read_b128 v[188:191], v225 offset:49152\n\t"
    "s_waitcnt lgkmcnt(12)\n\t"
    "v_add_f32 v128, v128, v136\n\t"
    "v_add_f32 v129, v129, v137\n\t"
    "v_add_f32 v130, v130, v138\n\t"
    "v_add_f32 v131, v131, v139\n\t"
    "v_add_f32 v132, v132, v140\n\t"
    "v_add_f32 v133, v133, v141\n\t"
    "v_add_f32 v134, v134, v142\n\t"
    "v_add_f32 v135, v135, v143\n\t"
    "s_waitcnt lgkmcnt(10)\n\t"
    "v_add_f32 v128, v128, v144\n\t"
    "v_add_f32 v129, v129, v145\n\t"
    "v_add_f32 v130, v130, v146\n\t"
    "v_add_f32 v131, v131, v147\n\t"
    "v_add_f32 v132, v132, v148\n\t"
    "v_add_f32 v133, v133, v149\n\t"
    "v_add_f32 v134, v134, v150\n\t"
    "v_add_f32 v135, v135, v151\n\t"
    "s_waitcnt lgkmcnt(8)\n\t"
    "v_add_f32 v128, v128, v152\n\t"
    "v_add_f32 v129, v129, v153\n\t"
    "v_add_f32 v130, v130, v154\n\t"
    "v_add_f32 v131, v131, v155\n\t"
    "v_add_f32 v132, v132, v156\n\t"
    "v_add_f32 v133, v133, v157\n\t"
    "v_add_f32 v134, v134, v158\n\t"
    "v_add_f32 v135, v135, v159\n\t"
    "s_waitcnt lgkmcnt(6)\n\t"
    "v_fma_f32 v128, v128, v251, v160\n\t"
    "v_fma_f32 v129, v129, v251, v161\n\t"
    "v_fma_f32 v130, v130, v251, v162\n\t"
    "v_fma_f32 v131, v131, v251, v163\n\t"
    "v_fma_f32 v132, v132, v251, v164\n\t"
    "v_fma_f32 v133, v133, v251, v165\n\t"
    "v_fma_f32 v134, v134, v251, v166\n\t"
    "v_fma_f32 v135, v135, v251, v167\n\t"
    "s_waitcnt lgkmcnt(4)\n\t"
    "v_add_f32 v128, v128, v168\n\t"
    "v_add_f32 v129, v129, v169\n\t"
    "v_add_f32 v130, v130, v170\n\t"
    "v_add_f32 v131, v131, v171\n\t"
    "v_add_f32 v132, v132, v172\n\t"
    "v_add_f32 v133, v133, v173\n\t"
    "v_add_f32 v134, v134, v174\n\t"
    "v_add_f32 v135, v135, v175\n\t"
    "s_waitcnt lgkmcnt(2)\n\t"
    "v_add_f32 v128, v128, v176\n\t"
    "v_add_f32 v129, v129, v177\n\t"
    "v_add_f32 v130, v130, v178\n\t"
    "v_add_f32 v131, v131, v179\n\t"
    "v_add_f32 v132, v132, v180\n\t"
    "v_add_f32 v133, v133, v181\n\t"
    "v_add_f32 v134, v134, v182\n\t"
    "v_add_f32 v135, v135, v183\n\t"
    "s_waitcnt lgkmcnt(0)\n\t"
    "v_add_f32 v128, v128, v184\n\t"
    "v_add_f32 v129, v129, v185\n\t"
    "v_add_f32 v130, v130, v186\n\t"
    "v_add_f32 v131, v131, v187\n\t"
    "v_add_f32 v132, v132, v188\n\t"
    "v_add_f32 v133, v133, v189\n\t"
    "v_add_f32 v134, v134, v190\n\t"
    "v_add_f32 v135, v135, v191\n\t"
    "v_cvt_pk_bf16_f32 v136, v128, v129\n\t"
    "v_cvt_pk_bf16_f32 v137, v130, v131\n\t"
    "v_cvt_pk_bf16_f32 v138, v132, v133\n\t"
    "v_cvt_pk_bf16_f32 v139, v134, v135\n\t"
    "global_store_dwordx4 v226, v[136:139], s[76:77]\n\t"
    "s_branch Lend_%=\n\t"
    "Lre0_%=:\n\t"
    "s_add_u32 s81, s81, 1\n\t"
    "s_cmp_gt_u32 s81, 0x60000\n\t"
    "s_cbranch_scc1 Lgo0_%=\n\t"
    "buffer_load_dwordx4 v[192:195], v252, %[rw], s69 offen sc1\n\t"
    "s_waitcnt vmcnt(0)\n\t"
    "s_branch Lchk0_%=\n\t"
    "Lre1_%=:\n\t"
    "s_add_u32 s81, s81, 1\n\t"
    "s_cmp_gt_u32 s81, 0x60000\n\t"
    "s_cbranch_scc1 Lgo1_%=\n\t"
    "buffer_load_dwordx4 v[196:199], v252, %[rw], s69 offen offset:1024 sc1\n\t"
    "s_waitcnt vmcnt(0)\n\t"
    "s_branch Lchk1_%=\n\t"
    "Lre2_%=:\n\t"
    "s_add_u32 s81, s81, 1\n\t"
    "s_cmp_gt_u32 s81, 0x60000\n\t"
    "s_cbranch_scc1 Lgo2_%=\n\t"
    "buffer_load_dwordx4 v[200:203], v252, %[rw], s69 offen offset:2048 sc1\n\t"
    "s_waitcnt vmcnt(0)\n\t"
    "s_branch Lchk2_%=\n\t"
    "Lre3_%=:\n\t"
    "s_add_u32 s81, s81, 1\n\t"
    "s_cmp_gt_u32 s81, 0x60000\n\t"
    "s_cbranch_scc1 Lgo3_%=\n\t"
    "buffer_load_dwordx4 v[204:207], v252, %[rw], s69 offen offset:3072 sc1\n\t"
    "s_waitcnt vmcnt(0)\n\t"
    "s_branch Lchk3_%=\n\t"
    "Lre4_%=:\n\t"
    "s_add_u32 s81, s81, 1\n\t"
    "s_cmp_gt_u32 s81, 0x60000\n\t"
    "s_cbranch_scc1 Lgo4_%=\n\t"
    "buffer_load_dwordx4 v[208:211], v252, %[rw], s80 offen sc1\n\t"
    "s_waitcnt vmcnt(0)\n\t"
    "s_branch Lchk4_%=\n\t"
    "Lre5_%=:\n\t"
    "s_add_u32 s81, s81, 1\n\t"
    "s_cmp_gt_u32 s81, 0x60000\n\t"
    "s_cbranch_scc1 Lgo5_%=\n\t"
    "buffer_load_dwordx4 v[212:215], v252, %[rw], s80 offen offset:1024 sc1\n\t"
    "s_waitcnt vmcnt(0)\n\t"
    "s_branch Lchk5_%=\n\t"
    "Lre6_%=:\n\t"
    "s_add_u32 s81, s81, 1\n\t"
    "s_cmp_gt_u32 s81, 0x60000\n\t"
    "s_cbranch_scc1 Lgo6_%=\n\t"
    "buffer_load_dwordx4 v[216:219], v252, %[rw], s80 offen offset:2048 sc1\n\t"
    "s_waitcnt vmcnt(0)\n\t"
    "s_branch Lchk6_%=\n\t"
    "Lre7_%=:\n\t"
    "s_add_u32 s81, s81, 1\n\t"
    "s_cmp_gt_u32 s81, 0x60000\n\t"
    "s_cbranch_scc1 Lgo7_%=\n\t"
    "buffer_load_dwordx4 v[220:223], v252, %[rw], s80 offen offset:3072 sc1\n\t"
    "s_waitcnt vmcnt(0)\n\t"
    "s_branch Lchk7_%=\n\t"
    "Lend_%=:\n\t"
    "s_nop 1\n\t"
    : "+{v[0:15]}"(d00), "+{v[16:31]}"(d01), "+{v[32:47]}"(d10), "+{v[48:63]}"(d11), "+{v244}"(h0), "+{v245}"(h1)
    : "{v[64:79]}"(wo0), "{v[80:95]}"(wo1), "{v[96:111]}"(wo2), "{v[112:127]}"(wo3), "{v252}"(l16), "{v248}"(hv), "{v251}"(gr), [rw] "s"(rw), [wv] "s"(w),
      "{s68}"(hc), "{s69}"(ob), "{s70}"(mring), "{s[76:77]}"(op), "{s[84:85]}"(ep)
    : "memory", "vcc", "scc",
        "v128", "v129", "v130", "v131", "v132", "v133", "v134", "v135", "v136", "v137", "v138", "v139", "v140", "v141", "v142", "v143", "v144", "v145",
        "v146", "v147", "v148", "v149", "v150", "v151", "v152", "v153", "v154", "v155", "v156", "v157", "v158", "v159", "v160", "v161", "v162", "v163",
        "v164", "v165", "v166", "v167", "v168", "v169", "v170", "v171", "v172", "v173", "v174", "v175", "v176", "v177", "v178", "v179", "v180", "v181",
        "v182", "v183", "v184", "v185", "v186", "v187", "v188", "v189", "v190", "v191", "v192", "v193", "v194", "v195", "v196", "v197", "v198", "v199",
        "v200", "v201", "v202", "v203", "v204", "v205", "v206", "v207", "v208", "v209", "v210", "v211", "v212", "v213", "v214", "v215", "v216", "v217",
        "v218", "v219", "v220", "v221", "v222", "v223", "v224", "v225", "v226", "v227", "v228", "v229", "v230", "v231", "v232", "v233", "v234", "v235",
        "v236", "v237", "v238", "v239", "s80", "s81", "s82", "s83", "s86", "s87", "s88", "s89", "s90", "s91", "s92", "s93", "s94", "s95", "s96", "s97"
  );
}

extern "C" void kernel_launch(void* const* d_in, const int* in_sizes, int n_in, void* d_out, int out_size, void* d_ws, size_t ws_size,
                              hipStream_t stream) {
  (void)in_sizes; (void)n_in; (void)out_size; (void)ws_size;
  fused_kernel<<<256, 512, 0, stream>>>(d_in[0], d_in[1], d_in[2], d_in[4], d_in[5], d_in[6], d_in[7], d_out, (char*)d_ws);
}
```
